```python
import torch

def _softmax(x, axis=-1):
    return torch.softmax(x, dim=axis)

def _gelu(x):
    return torch.nn.functional.gelu(x, approximate='tanh')

def _silu(x):
    return torch.nn.functional.silu(x)

def _sigmoid(x):
    return torch.sigmoid(x)

def _layernorm(x, axis=-1, eps=1e-05):
    m = x.mean(dim=axis, keepdim=True)
    v = x.var(dim=axis, keepdim=True, unbiased=False)
    return (x - m) / torch.sqrt(v + eps)

def _scatter_add(tgt, idx, src, axis=0):
    return tgt.clone().index_add_(axis, idx, src)

def _segment_sum(src, seg, n):
    out = torch.zeros((n,) + src.shape[1:], dtype=src.dtype)
    return out.index_add_(0, seg, src)

def _pad(x, pw):
    flat = tuple((v for pair in reversed(pw) for v in pair))
    return torch.nn.functional.pad(x, flat)
kernel_name = 'recurrent_scan__memory_aug_recurrent_mem_tr__glu_gated_bilinear__prompt'

def setup_inputs():

    _w = lambda a: (lambda t: t.to(torch.bfloat16) if t.is_floating_point() else t)(torch.as_tensor(a))

    def _A_setup():
        B, S, Dm = (1, 2048, 640)
        H, Dh = (32, 192)
        M, seg = (8, 32)
        seg = min(seg, S)
        S = S // seg * seg
        return {'x': _w((lambda *s: torch.randn(*s, dtype=torch.bfloat16) * 0.1)(B, S, Dm)), 'mem0': _w((lambda *s: torch.randn(*s, dtype=torch.bfloat16) * 1.6)(B, M, Dm)), 'Wq': _w((lambda *s: torch.randn(*s, dtype=torch.bfloat16) * 0.8)(Dm, H, Dh) * Dm ** (-0.5)), 'Wk': _w((lambda *s: torch.randn(*s, dtype=torch.bfloat16) * 0.8)(Dm, H, Dh) * Dm ** (-0.5)), 'Wv': _w((lambda *s: torch.randn(*s, dtype=torch.bfloat16) * 1.6)(Dm, H, Dh) * Dm ** (-0.5)), 'Wo': _w((lambda *s: torch.randn(*s, dtype=torch.bfloat16) * 1.6)(H, Dh, Dm) * (H * Dh) ** (-0.5)), 'seg_len': seg}
    _A_in = _A_setup()

    def _B_setup():
        B, S, D, F = (1, 2048, 640, 1536)
        return {'x': _w((lambda *s: torch.randn(*s, dtype=torch.bfloat16) * 0.1)(B, S, D)), 'w_gate': _w((lambda *s: torch.randn(*s, dtype=torch.bfloat16) * 0.4)(D, F) * D ** (-0.5)), 'w_up': _w((lambda *s: torch.randn(*s, dtype=torch.bfloat16) * 0.4)(D, F) * D ** (-0.5)), 'w_down': _w((lambda *s: torch.randn(*s, dtype=torch.bfloat16) * 0.4)(F, D) * F ** (-0.5))}
    _B_in = _B_setup()
    return {'A_x': _A_in['x'], 'A_mem0': _A_in['mem0'], 'A_Wq': _A_in['Wq'], 'A_Wk': _A_in['Wk'], 'A_Wv': _A_in['Wv'], 'A_Wo': _A_in['Wo'], 'A_seg_len': _A_in['seg_len'], 'B_x': _B_in['x'], 'B_w_gate': _B_in['w_gate'], 'B_w_up': _B_in['w_up'], 'B_w_down': _B_in['w_down']}

def reference(A_x, A_mem0, A_Wq, A_Wk, A_Wv, A_Wo, A_seg_len, B_x, B_w_gate, B_w_up, B_w_down):

    def _A_ref(x, mem0, Wq, Wk, Wv, Wo, seg_len):
        B, S, Dm = x.shape
        H, Dh = (Wq.shape[1], Wq.shape[2])
        M = mem0.shape[1]
        scale = Dh ** (-0.5)
        n_seg = S // seg_len
        L = M + seg_len
        idx = torch.arange(L)
        mask = (idx[:, None] < M) | (idx[:, None] >= idx[None, :])
        mem = mem0
        outs = []
        for s in range(n_seg):
            xs = x[:, s * seg_len:(s + 1) * seg_len, :].clone()
            h = torch.concatenate([mem, xs], axis=1)
            q = torch.einsum('btd,dhe->bhte', h, Wq)
            k = torch.einsum('btd,dhe->bhte', h, Wk)
            v = torch.einsum('btd,dhe->bhte', h, Wv)
            scores = torch.einsum('bhqe,bhke->bhqk', q, k) * scale
            scores = torch.where(mask[None, None], scores, -1000000000.0)
            p = _softmax(scores, axis=-1)
            o = torch.einsum('bhqk,bhke->bhqe', p, v)
            o = torch.einsum('bhte,hed->btd', o, Wo)
            mem = o[:, :M, :].clone()
            outs.append(o[:, M:, :])
        return torch.concatenate(outs, axis=1)

    def _B_ref(x, w_gate, w_up, w_down):
        g = torch.einsum('bsd,df->bsf', x, w_gate)
        u = torch.einsum('bsd,df->bsf', x, w_up)
        h = g * u
        return torch.einsum('bsf,fd->bsd', h, w_down)
    x = A_x
    xn = _layernorm(x, axis=-1)
    s = _A_ref(xn, A_mem0, A_Wq, A_Wk, A_Wv, A_Wo, A_seg_len)
    g = _silu(_B_ref(xn, B_w_gate, B_w_up, B_w_down))
    return x + s * g
```

```cpp
#include <hip/hip_runtime.h>
#include <stdint.h>
#include <type_traits>
typedef __attribute__((ext_vector_type(8), may_alias)) __bf16 bf16x8;
typedef __attribute__((ext_vector_type(16), may_alias)) __bf16 bf16x16;
typedef __attribute__((ext_vector_type(4), may_alias)) __bf16 bf16x4;
typedef __attribute__((ext_vector_type(2), may_alias)) __bf16 bf16x2;
typedef __attribute__((ext_vector_type(4), may_alias)) float f32x4;
typedef __attribute__((ext_vector_type(2), may_alias)) float f32x2;
typedef __attribute__((ext_vector_type(4), may_alias)) unsigned u32x4;
typedef __attribute__((ext_vector_type(2), may_alias)) unsigned u32x2;
typedef __amdgpu_buffer_rsrc_t rsrc_t;
#define LAS __attribute__((address_space(3)))
#define SENT 0xFFFFFFFFu
#define DEVI __device__ __forceinline__

constexpr int NSEG = 64;
constexpr unsigned OFF_FLAGS = 0;
constexpr unsigned OFF_MEMB = 1u << 20;
constexpr unsigned OFF_EBUF = OFF_MEMB + 768 * 1024;
constexpr unsigned OFF_PMB = OFF_EBUF + (1u << 20);
constexpr unsigned OFF_ALB = OFF_PMB + 256 * 1024;
constexpr unsigned SENT_END = OFF_ALB + 64 * 1024;
constexpr unsigned OFF_XNF = 4u << 20;
constexpr unsigned OFF_QF = 8u << 20;
constexpr unsigned SZ_QF = 64u * 192 * 2 * 1024;
constexpr unsigned OFF_KF = OFF_QF + SZ_QF;
constexpr unsigned OFF_VF = OFF_QF + 2 * SZ_QF;
constexpr unsigned OFF_KM = OFF_QF + 3 * SZ_QF;
constexpr unsigned OFF_BF = OFF_KM + 64 * 32 * 6 * 512;
constexpr unsigned OFF_Z = OFF_BF + 80 * 16 * 10 * 2048;
constexpr unsigned OFF_PX = OFF_Z + 64u * 80 * 32 * 5 * 128;
constexpr unsigned OFF_HF = OFF_PX + 64 * 160 * 512;
constexpr unsigned OFF_G = OFF_HF + 64 * 96 * 1024;
constexpr unsigned OFF_TS = OFF_G + 2048 * 640 * 2;
constexpr unsigned OFF_REC = 32768;
constexpr unsigned OFF_YF = 65536;
constexpr unsigned FLAGS_END = 131072;
constexpr int N_S = 32, N_V = 80, N_QKV = 58, N_VO = 32, N_GU = 12, N_DN = 5, N_TK = 37;

struct Params {
  const uint16_t* x; const uint16_t* mem0;
  const uint16_t* Wq; const uint16_t* Wk; const uint16_t* Wv; const uint16_t* Wo;
  const uint16_t* Wg; const uint16_t* Wu; const uint16_t* Wd;
  uint16_t* out;
  char* ws;
  int nsteps; int dS, dVm, dVe, dVl, inc, dec; unsigned mask;
};

DEVI u32x4 ld_sc1(rsrc_t r, unsigned off) { return __builtin_amdgcn_raw_buffer_load_b128(r, off, 0, 16); }
DEVI unsigned ld1_sc1(rsrc_t r, unsigned off) { return __builtin_amdgcn_raw_buffer_load_b32(r, off, 0, 16); }
DEVI u32x4 ld_plain(rsrc_t r, unsigned off) { return __builtin_amdgcn_raw_buffer_load_b128(r, off, 0, 0); }
DEVI void st_sc1(rsrc_t r, unsigned off, u32x4 v) { __builtin_amdgcn_raw_buffer_store_b128(v, r, off, 0, 16); }
DEVI void st2_sc1(rsrc_t r, unsigned off, u32x2 v) { __builtin_amdgcn_raw_buffer_store_b64(v, r, off, 0, 16); }
DEVI void st1_sc1(rsrc_t r, unsigned off, unsigned v) { __builtin_amdgcn_raw_buffer_store_b32(v, r, off, 0, 16); }
DEVI unsigned mx4(u32x4 v) { return max(max(v.x, v.y), max(v.z, v.w)); }
DEVI unsigned pk2(float a, float b) { bf16x2 v = {(__bf16)a, (__bf16)b}; return __builtin_bit_cast(unsigned, v); }
DEVI unsigned san(unsigned v) { return min(v, 0xFFFFFFFEu); }
DEVI bf16x4 trread(LAS char* p) { return __builtin_amdgcn_ds_read_tr16_b64_v4bf16((LAS bf16x4*)p); }
DEVI float fmx(float a, float b) { float r; asm("v_max_f32 %0, %1, %2" : "=v"(r) : "v"(a), "v"(b)); return r; }
DEVI float fmx3(float a, float b, float c) { float r; asm("v_max3_f32 %0, %1, %2, %3" : "=v"(r) : "v"(a), "v"(b), "v"(c)); return r; }
template <bool AG> DEVI void smf(f32x4& acc, bf16x8 a, const bf16x16& b, int idx) {
  if constexpr (AG) asm volatile("v_smfmac_f32_16x16x64_bf16 %0, %1, %2, %3" : "+v"(acc) : "v"(a), "a"(b), "v"(idx));
  else asm volatile("v_smfmac_f32_16x16x64_bf16 %0, %1, %2, %3" : "+v"(acc) : "v"(a), "v"(b), "v"(idx));
}
DEVI f32x4 zero4() { float a, b, c, d; asm volatile("v_mov_b32 %0, 0" : "=v"(a)); asm volatile("v_mov_b32 %0, 0" : "=v"(b)); asm volatile("v_mov_b32 %0, 0" : "=v"(c)); asm volatile("v_mov_b32 %0, 0" : "=v"(d)); return (f32x4){a, b, c, d}; }
DEVI float xor16max(float x) { auto r = __builtin_amdgcn_permlane16_swap(__float_as_uint(x), __float_as_uint(x), false, false); unsigned a = r[0], b = r[1]; return fmx(__uint_as_float(a), __uint_as_float(b)); }
DEVI float xor32max(float x) { auto r = __builtin_amdgcn_permlane32_swap(__float_as_uint(x), __float_as_uint(x), false, false); unsigned a = r[0], b = r[1]; return fmx(__uint_as_float(a), __uint_as_float(b)); }
DEVI float xor16add(float x) { auto r = __builtin_amdgcn_permlane16_swap(__float_as_uint(x), __float_as_uint(x), false, false); unsigned a = r[0], b = r[1]; return __uint_as_float(a) + __uint_as_float(b); }
DEVI float xor32add(float x) { auto r = __builtin_amdgcn_permlane32_swap(__float_as_uint(x), __float_as_uint(x), false, false); unsigned a = r[0], b = r[1]; return __uint_as_float(a) + __uint_as_float(b); }
DEVI f32x4 halfsum2(f32x4 A, f32x4 B) {
  f32x4 o;
#pragma unroll
  for (int r = 0; r < 4; r++) {
    float a = A[r], b = B[r];
    auto s = __builtin_amdgcn_permlane32_swap(__float_as_uint(a), __float_as_uint(b), false, false);
    unsigned s0 = s[0], s1 = s[1];
    o[r] = __uint_as_float(s0) + __uint_as_float(s1);
  }
  return o;
}
#define STAMP(role, k)
#define STAMP_DECL
#define STAMP_BEGIN
#define STAMP_FLUSH(role)
#define TL(id, cond)
#define GST(role, seg, k, cond)
constexpr unsigned SPIN_CAP = 1u << 22;

DEVI unsigned now32() { unsigned long long t; asm volatile("s_memtime %0\n\ts_waitcnt lgkmcnt(0)" : "=s"(t) :: "memory"); return (unsigned)t; }
DEVI void wait_until(unsigned t) { while ((int)(now32() - t) < 0) { } }
DEVI void adapt(int& D, bool fail, int inc, int dec) { D += fail ? inc : -dec; }

DEVI bool gather_mem(rsrc_t ws, int s, LAS char* ml, int w, int lane) {
  unsigned base = OFF_MEMB + s * 10240 + lane * 16;
  unsigned spins = 0;
  if (w < 2) {
    u32x4 v0, v1, v2;
    while (true) {
      asm volatile("" ::: "memory");
      v0 = ld_sc1(ws, base + w * 1024); v1 = ld_sc1(ws, base + (w + 4) * 1024); v2 = ld_sc1(ws, base + (w + 8) * 1024);
      __builtin_amdgcn_sched_barrier(0);
      unsigned m = max(max(mx4(v0), mx4(v1)), mx4(v2));
      if (__all(m != SENT) || spins > SPIN_CAP) break;
      ++spins;
    }
    *(LAS u32x4*)(ml + w * 1024 + lane * 16) = v0;
    *(LAS u32x4*)(ml + (w + 4) * 1024 + lane * 16) = v1;
    *(LAS u32x4*)(ml + (w + 8) * 1024 + lane * 16) = v2;
  } else {
    u32x4 v0, v1;
    while (true) {
      asm volatile("" ::: "memory");
      v0 = ld_sc1(ws, base + w * 1024); v1 = ld_sc1(ws, base + (w + 4) * 1024);
      __builtin_amdgcn_sched_barrier(0);
      unsigned m = max(mx4(v0), mx4(v1));
      if (__all(m != SENT) || spins > SPIN_CAP) break;
      ++spins;
    }
    *(LAS u32x4*)(ml + w * 1024 + lane * 16) = v0;
    *(LAS u32x4*)(ml + (w + 4) * 1024 + lane * 16) = v1;
  }
  return spins != 0;
}
#define SB() __builtin_amdgcn_sched_barrier(0)
DEVI bf16x16 cat16(bf16x8 lo, bf16x8 hi) { return __builtin_shufflevector(lo, hi, 0, 1, 2, 3, 4, 5, 6, 7, 8, 9, 10, 11, 12, 13, 14, 15); }

DEVI bf16x16 tr_frag(LAS char* tile, int ldb, int c0, int lane) {
  const int bg = lane >> 4, q4 = (lane & 15) >> 2, p4 = lane & 3;
  bf16x4 e4[4];
#pragma unroll
  for (int jj = 0; jj < 4; jj++) {
    int g = 2 * (jj >> 1) + (bg >> 1), pp = 2 * (bg & 1) + (jj & 1);
    int row = 8 * g + 2 * pp + (q4 & 1) + 32 * (q4 >> 1);
    e4[jj] = trread(tile + row * ldb + (c0 + 4 * p4) * 2);
  }
  bf16x16 f;
#pragma unroll
  for (int jj = 0; jj < 4; jj++)
#pragma unroll
    for (int e = 0; e < 4; e++) f[4 * jj + e] = e4[jj][e];
  return f;
}

constexpr int PF = 3;
struct MMState { bf16x8 a[PF + 1]; bf16x16 fb[PF + 1]; };
DEVI void mm_pre_fb(MMState& st, LAS char* wl, int lane) {
#pragma unroll
  for (int q = 0; q < PF; q++) st.fb[q] = cat16(*(LAS bf16x8*)(wl + q * 2048 + lane * 16), *(LAS bf16x8*)(wl + q * 2048 + 1024 + lane * 16));
}
DEVI void mm_pre_a(MMState& st, LAS char* ml, int aoff) {
#pragma unroll
  for (int q = 0; q < PF; q++) st.a[q] = *(LAS bf16x8*)(ml + q * 1024 + aoff);
}
template <int NR, int T0, int WT, bool LDST, bool ENDNOP, class F>
DEVI void mm_phase(f32x4 (&acc)[4], MMState& st, const bf16x16 (&W)[WT][10], LAS char* ml, int aoff, LAS char* wl, int idx, int lane, F filler) {
  constexpr int NT = NR + (LDST ? 1 : 0);
#pragma unroll
  for (int t = 0; t < 4; t++) acc[t] = (f32x4){0, 0, 0, 0};
  if constexpr (NT == 3) asm volatile("s_nop 1" : "+v"(acc[0]), "+v"(acc[1]), "+v"(acc[2]));
  else asm volatile("s_nop 1" : "+v"(acc[0]), "+v"(acc[1]), "+v"(acc[2]), "+v"(acc[3]));
  bf16x8 flo, fhi;
#pragma unroll
  for (int kk = 0; kk < 10; kk++) {
    const int cu = kk % (PF + 1), nx = (kk + PF) % (PF + 1);
    const bool pf = kk + PF < 10;
#pragma unroll
    for (int t = 0; t < NT; t++) {
      if (t < NR) {
        if ((T0 + t) * 10 + kk < 32) smf<true>(acc[t], st.a[cu], W[T0 + (t < NR ? t : 0)][kk], idx);
        else smf<false>(acc[t], st.a[cu], W[T0 + (t < NR ? t : 0)][kk], idx);
      } else smf<false>(acc[t], st.a[cu], st.fb[cu], idx);
      SB();
      if (pf) {
        if (t == 0 && LDST) flo = *(LAS bf16x8*)(wl + (kk + PF) * 2048 + lane * 16);
        if (t == 1 && LDST) fhi = *(LAS bf16x8*)(wl + (kk + PF) * 2048 + 1024 + lane * 16);
      }
      filler(kk, t);
      if (t == NT - 1 && pf) { st.a[nx] = *(LAS bf16x8*)(ml + (kk + PF) * 1024 + aoff); if (LDST) st.fb[nx] = cat16(flo, fhi); }
      SB();
    }
  }
  if constexpr (ENDNOP) {
    if constexpr (NT == 3) asm volatile("s_nop 7" : "+v"(acc[0]), "+v"(acc[1]), "+v"(acc[2]));
    else asm volatile("s_nop 7" : "+v"(acc[0]), "+v"(acc[1]), "+v"(acc[2]), "+v"(acc[3]));
  }
}
struct NoFill { DEVI void operator()(int, int) const {} };
DEVI void lds_signal(LAS char* c, int w, unsigned v) { asm volatile("" ::: "memory"); *(volatile LAS unsigned*)(c + w * 4) = v; asm volatile("" ::: "memory"); }
DEVI void lds_wait3(LAS char* c, unsigned target) {
  while (true) {
    asm volatile("" ::: "memory");
    u32x4 v = *(volatile LAS u32x4*)c;
    unsigned m = min(min(v.y, v.z), v.w);
    if (__builtin_amdgcn_readfirstlane(m) >= target) break;
  }
  asm volatile("" ::: "memory");
}
constexpr unsigned OOB = 0x80000000u;

DEVI void glds16(const void* gsrc, unsigned lds_dst) {
  unsigned keep;
  asm volatile("s_mov_b32 %0, m0\n\ts_mov_b32 m0, %2\n\ts_nop 0\n\tglobal_load_lds_dwordx4 %1, off\n\ts_mov_b32 m0, %0" : "=&s"(keep) : "v"(gsrc), "s"(lds_dst) : "memory");
}
DEVI void drain() { asm volatile("s_waitcnt vmcnt(0)" ::: "memory"); }
DEVI void atom_add(char* ws, unsigned off, unsigned v) { __hip_atomic_fetch_add((unsigned*)(ws + off), v, __ATOMIC_RELAXED, __HIP_MEMORY_SCOPE_AGENT); }
DEVI void wait_flags(rsrc_t ws, unsigned off, unsigned target) {
  unsigned spins = 0;
  while (true) {
    asm volatile("" ::: "memory");
    unsigned v = ld1_sc1(ws, off);
    if (__all(v >= target) || spins > SPIN_CAP) break;
    ++spins;
    __builtin_amdgcn_s_sleep(2);
  }
  asm volatile("" ::: "memory");
}

DEVI void role_S(const Params& p, int h, LAS char* smem, rsrc_t ws) {
  const int w = __builtin_amdgcn_readfirstlane(threadIdx.x >> 6);
  if (w == 0) {
    int tid = threadIdx.x; asm volatile("; roleSA" : "+v"(tid) :: "memory");

  const int lane = tid & 63;
  const bool stampme = (h == 0);
  LAS char* wl = smem;
  LAS char* stg = smem + 122880;
  LAS char* ml = smem + 122880;
  LAS char* qk = ml + 10240;
  LAS char* cnt = qk + 6144;
  LAS char* wlw = wl + (w - 1) * 40960;
  u32x4 pre[6];
  rsrc_t rq = __builtin_amdgcn_make_buffer_rsrc((void*)p.Wq, 0, 640 * 6144 * 2, 0x00020000);
  rsrc_t rk = __builtin_amdgcn_make_buffer_rsrc((void*)p.Wk, 0, 640 * 6144 * 2, 0x00020000);
  auto issue = [&](int it) {
    const int mat = it / 10, kk = it % 10;
#pragma unroll
    for (int i = 0; i < 6; i++) {
      int u = i * 256 + tid; int row = u / 24, pc = u - row * 24;
      pre[i] = __builtin_amdgcn_raw_buffer_load_b128(mat ? rk : rq, row * 12288 + pc * 16, kk * 64 * 12288 + h * 384, 0);
    }
  };
  const int idx = (lane & 8) ? 0xEEEE : 0x4444;
  const int aoff = ((lane >> 3) & 1) * 512 + (lane >> 4) * 128 + (lane & 7) * 16;
  const float cs = 0.07216878364870322f * 1.4426950408889634f;
  int D = p.dS;
  unsigned tref = now32();
    bf16x16 W[3][10];
    issue(0);
#pragma unroll
    for (int it = 0; it < 20; it++) {
      const int mat = it / 10, kk = it % 10;
#pragma unroll
      for (int i = 0; i < 6; i++) *(LAS u32x4*)(stg + (i * 256 + tid) * 16) = pre[i];
      __syncthreads();
      if (it < 19) issue(it + 1);
      if (mat == 0) {
#pragma unroll
        for (int t = 0; t < 3; t++) W[t][kk] = tr_frag(stg, 384, 16 * t, lane);
      }
      __syncthreads();
    }
    tref = now32();
    *(LAS u32x4*)cnt = (u32x4){0, 0, 0, 0};
    LAS char* q2addr = (lane < 32) ? qk + (32 + (lane & 15)) * 16 + ((lane >> 4) & 1) * 8 : cnt + 64 + (lane & 31) * 8;
    const unsigned eoff = ((lane & 15) < 8) ? (unsigned)((lane >> 4) * 128 + (lane & 7) * 16) : OOB;
    const unsigned poff = ((lane & 15) < 8 && lane < 32) ? (unsigned)((lane & 7) * 16 + (lane >> 4) * 8) : OOB;
    const unsigned aloff = (lane < 8) ? (unsigned)(lane * 4) : OOB;
    TL(1, h == 0);
    u32x4 kx[12];
    const unsigned kfoff = lane == 0 ? (unsigned)((32 + h) * 4) : OOB, kftgt = lane == 0 ? 6u : 0u;
    wait_flags(ws, kfoff, kftgt);
#pragma unroll
    for (int i = 0; i < 12; i++) kx[i] = ld_sc1(ws, OFF_KF + (0 * 32 + h) * 12288 + i * 1024 + lane * 16);
    drain(); TL(2, h == 0);
    const int g = lane >> 4, q4 = (lane & 15) >> 2, p4 = lane & 3;
    for (int s = 0; s < p.nsteps; s++) {
      STAMP_DECL STAMP_BEGIN STAMP(w, 0);
      if (s > 0) wait_until(tref + D);
      bool fail = gather_mem(ws, s, ml, w, lane);
      if (s > 0) adapt(D, fail, p.inc, p.dec);
      STAMP(w, 1);
      __syncthreads();
      STAMP(w, 2);
      f32x4 acc[4]; MMState st;
      mm_pre_a(st, ml, aoff);
      mm_phase<3, 0, 3, false, true>(acc, st, W, ml, aoff, wl, idx, lane, NoFill());
      STAMP(w, 3);
      {
        f32x4 o = halfsum2(acc[0], acc[1]);
        int e = 16 * (lane >> 5) + (lane & 15);
        u32x2 v = {pk2(o[0], o[1]), pk2(o[2], o[3])};
        *(LAS u32x2*)(qk + e * 16 + ((lane >> 4) & 1) * 8) = v;
        f32x4 o2 = halfsum2(acc[2], acc[2]);
        u32x2 v2 = {pk2(o2[0], o2[1]), pk2(o2[2], o2[3])};
        *(LAS u32x2*)(q2addr) = v2;
      }
      if (s > 0) { drain(); if (lane == 0) atom_add(p.ws, ((s - 1) * 128 + 96 + h) * 4, 1); }
      lds_wait3(cnt, s + 1);
      STAMP(w, 4);
      bf16x8 qf[6];
#pragma unroll
      for (int ks = 0; ks < 6; ks++) {
        int e0 = 32 * ks + 8 * g + q4;
        bf16x4 qa = trread(qk + e0 * 16 + (p4 & 1) * 8), qb = trread(qk + (e0 + 4) * 16 + (p4 & 1) * 8);
        qf[ks] = __builtin_shufflevector(qa, qb, 0, 1, 2, 3, 4, 5, 6, 7);
      }
      f32x4 T1, T2;
      asm volatile("s_nop 1\n\tv_mfma_f32_16x16x32_bf16 %0, %2, %8, 0\n\tv_mfma_f32_16x16x32_bf16 %1, %3, %8, 0\n\t"
                   "v_mfma_f32_16x16x32_bf16 %0, %4, %9, %0\n\tv_mfma_f32_16x16x32_bf16 %1, %5, %9, %1\n\t"
                   "v_mfma_f32_16x16x32_bf16 %0, %6, %10, %0\n\tv_mfma_f32_16x16x32_bf16 %1, %7, %10, %1"
                   : "=&v"(T1), "=&v"(T2) : "v"(kx[0]), "v"(kx[1]), "v"(kx[2]), "v"(kx[3]), "v"(kx[4]), "v"(kx[5]), "v"(qf[0]), "v"(qf[1]), "v"(qf[2]));
      asm volatile("s_nop 1\n\tv_mfma_f32_16x16x32_bf16 %0, %2, %8, %0\n\tv_mfma_f32_16x16x32_bf16 %1, %3, %8, %1\n\t"
                   "v_mfma_f32_16x16x32_bf16 %0, %4, %9, %0\n\tv_mfma_f32_16x16x32_bf16 %1, %5, %9, %1\n\t"
                   "v_mfma_f32_16x16x32_bf16 %0, %6, %10, %0\n\tv_mfma_f32_16x16x32_bf16 %1, %7, %10, %1\n\ts_nop 7"
                   : "+v"(T1), "+v"(T2) : "v"(kx[6]), "v"(kx[7]), "v"(kx[8]), "v"(kx[9]), "v"(kx[10]), "v"(kx[11]), "v"(qf[3]), "v"(qf[4]), "v"(qf[5]));
      STAMP(w, 5);
      float mx = fmx3(fmx3(T1[0], T1[1], T1[2]), fmx3(T1[3], T2[0], T2[1]), fmx(T2[2], T2[3]));
      mx = xor32max(xor16max(mx));
      const float mxc = mx * cs;
      float E1[4], E2[4], lx = 0;
#pragma unroll
      for (int r = 0; r < 4; r++) { E1[r] = __builtin_amdgcn_exp2f(__builtin_fmaf(T1[r], cs, -mxc)); E2[r] = __builtin_amdgcn_exp2f(__builtin_fmaf(T2[r], cs, -mxc)); }
      {
        u32x4 ev = {san(pk2(E1[0], E1[1])), san(pk2(E1[2], E1[3])), san(pk2(E2[0], E2[1])), san(pk2(E2[2], E2[3]))};
        __builtin_amdgcn_raw_buffer_store_b128(ev, ws, eoff, OFF_EBUF + (s * 32 + h) * 512, 16);
      }
      STAMP(w, 6);
      lx = ((E1[0] + E1[1]) + (E1[2] + E1[3])) + ((E2[0] + E2[1]) + (E2[2] + E2[3]));
      lx = xor32add(xor16add(lx));
      __syncthreads();
      STAMP(w, 7);
      bf16x8 kmf[6];
#pragma unroll
      for (int ks = 0; ks < 6; ks++) {
        int e0 = 192 + 32 * ks + 8 * g + q4;
        bf16x4 ka = trread(qk + e0 * 16 + (p4 & 1) * 8), kb = trread(qk + (e0 + 4) * 16 + (p4 & 1) * 8);
        kmf[ks] = __builtin_shufflevector(ka, kb, 0, 1, 2, 3, 4, 5, 6, 7);
      }
      f32x4 T0;
      asm volatile("s_nop 1\n\tv_mfma_f32_16x16x32_bf16 %0, %1, %4, 0\n\tv_mfma_f32_16x16x32_bf16 %0, %2, %5, %0\n\tv_mfma_f32_16x16x32_bf16 %0, %3, %6, %0"
                   : "=&v"(T0) : "v"(kmf[0]), "v"(kmf[1]), "v"(kmf[2]), "v"(qf[0]), "v"(qf[1]), "v"(qf[2]));
      asm volatile("s_nop 1\n\tv_mfma_f32_16x16x32_bf16 %0, %1, %4, %0\n\tv_mfma_f32_16x16x32_bf16 %0, %2, %5, %0\n\tv_mfma_f32_16x16x32_bf16 %0, %3, %6, %0\n\ts_nop 7"
                   : "+v"(T0) : "v"(kmf[3]), "v"(kmf[4]), "v"(kmf[5]), "v"(qf[3]), "v"(qf[4]), "v"(qf[5]));
      float mm = xor16max(fmx(fmx3(T0[0], T0[1], T0[2]), T0[3]));
      float m = fmx(mx, mm);
      const float mc = m * cs;
      float ax = __builtin_amdgcn_exp2f(mxc - mc);
      float E0[4], lm;
#pragma unroll
      for (int r = 0; r < 4; r++) E0[r] = __builtin_amdgcn_exp2f(__builtin_fmaf(T0[r], cs, -mc));
      lm = xor16add((E0[0] + E0[1]) + (E0[2] + E0[3]));
      float inv = __builtin_amdgcn_rcpf(lx * ax + lm);
      {
        u32x2 pv = {san(pk2(E0[0] * inv, E0[1] * inv)), san(pk2(E0[2] * inv, E0[3] * inv))};
        __builtin_amdgcn_raw_buffer_store_b64(pv, ws, poff, OFF_PMB + (s * 32 + h) * 128, 16);
        __builtin_amdgcn_raw_buffer_store_b32(san(__float_as_uint(ax * inv)), ws, aloff, OFF_ALB + (s * 32 + h) * 32, 16);
      }
      tref = now32();
      STAMP(w, 8);
#pragma unroll
      for (int ks = 0; ks < 6; ks++) __builtin_amdgcn_raw_buffer_store_b128(__builtin_bit_cast(u32x4, kmf[ks]), ws, eoff, OFF_KM + ((s * 32 + h) * 6 + ks) * 512, 16);
      if (s + 1 < NSEG) {
        wait_flags(ws, kfoff == OOB ? OOB : kfoff + (s + 1) * 512, kftgt);
#pragma unroll
        for (int i = 0; i < 12; i++) kx[i] = ld_sc1(ws, OFF_KF + ((s + 1) * 32 + h) * 12288 + i * 1024 + lane * 16);
      }
      STAMP_FLUSH(w);
      if (lane == 0 && s == p.nsteps - 1) { unsigned* d = (unsigned*)(p.ws + OFF_TS + 40960 + h * 16); d[0] = D; }
      if (tid == 0 && h == 0) { long long t = wall_clock64(); __builtin_amdgcn_raw_buffer_store_b64(__builtin_bit_cast(u32x2, t), ws, OFF_TS + s * 8, 0, 0); }
    }
    drain(); if (lane == 0) atom_add(p.ws, ((p.nsteps - 1) * 128 + 96 + h) * 4, 1);
    __builtin_amdgcn_endpgm(); __builtin_unreachable();
  } else {
    int tid = threadIdx.x; asm volatile("; roleSB" : "+v"(tid) :: "memory");

  const int lane = tid & 63;
  const bool stampme = (h == 0);
  LAS char* wl = smem;
  LAS char* stg = smem + 122880;
  LAS char* ml = smem + 122880;
  LAS char* qk = ml + 10240;
  LAS char* cnt = qk + 6144;
  LAS char* wlw = wl + (w - 1) * 40960;
  u32x4 pre[6];
  rsrc_t rq = __builtin_amdgcn_make_buffer_rsrc((void*)p.Wq, 0, 640 * 6144 * 2, 0x00020000);
  rsrc_t rk = __builtin_amdgcn_make_buffer_rsrc((void*)p.Wk, 0, 640 * 6144 * 2, 0x00020000);
  auto issue = [&](int it) {
    const int mat = it / 10, kk = it % 10;
#pragma unroll
    for (int i = 0; i < 6; i++) {
      int u = i * 256 + tid; int row = u / 24, pc = u - row * 24;
      pre[i] = __builtin_amdgcn_raw_buffer_load_b128(mat ? rk : rq, row * 12288 + pc * 16, kk * 64 * 12288 + h * 384, 0);
    }
  };
  const int idx = (lane & 8) ? 0xEEEE : 0x4444;
  const int aoff = ((lane >> 3) & 1) * 512 + (lane >> 4) * 128 + (lane & 7) * 16;
  const float cs = 0.07216878364870322f * 1.4426950408889634f;
  int D = p.dS;
  unsigned tref = now32();
    LAS char* q01addr = qk + (48 * w + 16 * (lane >> 5) + (lane & 15)) * 16 + ((lane >> 4) & 1) * 8;
    LAS char* q2addr = (lane < 32) ? qk + (48 * w + 32 + (lane & 15)) * 16 + ((lane >> 4) & 1) * 8 : cnt + 64 + w * 256 + (lane & 31) * 8;
    bf16x16 W[5][10];
    issue(0);
#pragma unroll
    for (int it = 0; it < 20; it++) {
      const int mat = it / 10, kk = it % 10;
#pragma unroll
      for (int i = 0; i < 6; i++) *(LAS u32x4*)(stg + (i * 256 + tid) * 16) = pre[i];
      __syncthreads();
      if (it < 19) issue(it + 1);
      const int nt = mat == 0 ? 3 : 4;
#pragma unroll
      for (int t = 0; t < 4; t++) if (t < nt) {
        bf16x16 f = tr_frag(stg, 384, mat == 0 ? 16 * (3 * w + t) : 16 * (4 * (w - 1) + t), lane);
        if (t < nt - 1) W[mat == 0 ? t : 2 + t][kk] = f;
        else { LAS char* d = wlw + (mat * 10 + kk) * 2048 + lane * 16;
          *(LAS bf16x8*)d = __builtin_shufflevector(f, f, 0, 1, 2, 3, 4, 5, 6, 7);
          *(LAS bf16x8*)(d + 1024) = __builtin_shufflevector(f, f, 8, 9, 10, 11, 12, 13, 14, 15); }
      }
      __syncthreads();
    }
    tref = now32();
    for (int s = 0; s < p.nsteps; s++) {
      STAMP_DECL STAMP_BEGIN STAMP(w, 0);
      if (s > 0) wait_until(tref + D);
      MMState st;
      mm_pre_fb(st, wlw, lane);
      bool fail = gather_mem(ws, s, ml, w, lane);
      if (s > 0) adapt(D, fail, p.inc, p.dec);
      STAMP(w, 1);
      __syncthreads();
      STAMP(w, 2);
      f32x4 acc[4];
      mm_pre_a(st, ml, aoff);
      MMState st2;
      mm_phase<2, 0, 5, true, false>(acc, st, W, ml, aoff, wlw, idx, lane, [&](int kk, int t) {
        if (kk >= 7 && t == 2) { st2.a[kk - 7] = *(LAS bf16x8*)(ml + (kk - 7) * 1024 + aoff); }
        if (kk >= 7 && t == 0) { st2.fb[kk - 7] = cat16(*(LAS bf16x8*)(wlw + 20480 + (kk - 7) * 2048 + lane * 16), *(LAS bf16x8*)(wlw + 20480 + (kk - 7) * 2048 + 1024 + lane * 16)); }
      });
      STAMP(w, 3);
      f32x4 qa0 = acc[0], qa1 = acc[1], qa2 = acc[2];
      f32x4 acc2[4]; f32x4 o, o2; u32x2 v, v2;
      mm_phase<3, 2, 5, true, true>(acc2, st2, W, ml, aoff, wlw + 20480, idx, lane, [&](int kk, int t) {
        if (kk == 1 && t == 0) o = halfsum2(qa0, qa1);
        if (kk == 1 && t == 2) { v = (u32x2){pk2(o[0], o[1]), pk2(o[2], o[3])}; *(LAS u32x2*)(q01addr) = v; }
        if (kk == 2 && t == 0) o2 = halfsum2(qa2, qa2);
        if (kk == 2 && t == 2) { v2 = (u32x2){pk2(o2[0], o2[1]), pk2(o2[2], o2[3])}; *(LAS u32x2*)(q2addr) = v2; lds_signal(cnt, w, s + 1); }
      });
#pragma unroll
      for (int t = 0; t < 4; t++) acc[t] = acc2[t];
      STAMP(w, 5);
#pragma unroll
      for (int pr = 0; pr < 2; pr++) {
        f32x4 o = halfsum2(acc[2 * pr], acc[2 * pr + 1]);
        int e = 192 + 64 * (w - 1) + 32 * pr + 16 * (lane >> 5) + (lane & 15);
        u32x2 v = {pk2(o[0], o[1]), pk2(o[2], o[3])};
        *(LAS u32x2*)(qk + e * 16 + ((lane >> 4) & 1) * 8) = v;
      }
      __syncthreads();
      tref = now32() + 450;
      STAMP(w, 7);
      STAMP_FLUSH(w);
    }
    __builtin_amdgcn_endpgm(); __builtin_unreachable();
  }
}

DEVI void role_V(const Params& p, int j, LAS char* smem, rsrc_t ws) {
  int tid = threadIdx.x; asm volatile("; roleV" : "+v"(tid) :: "memory");
  const int lane = tid & 63, w = __builtin_amdgcn_readfirstlane(tid >> 6);
  const bool stampme = (j == 0);
  LAS char* ml = smem;
  LAS char* yl = smem + 10240;
  LAS char* red = smem + 10240 + 4096;
  bf16x16 B[4][10];
#pragma unroll
  for (int t = 0; t < 4; t++)
#pragma unroll
    for (int kk = 0; kk < 10; kk++) {
      unsigned o = OFF_BF + ((j * 16 + 4 * w + t) * 10 + kk) * 2048 + lane * 16;
      B[t][kk] = cat16(__builtin_bit_cast(bf16x8, ld_plain(ws, o)), __builtin_bit_cast(bf16x8, ld_plain(ws, o + 1024)));
    }
  const int idx = (lane & 8) ? 0xEEEE : 0x4444;
  const int half = (lane >> 3) & 1, g = lane >> 4, i8 = lane & 7;
  const int aoff = half * 512 + g * 128 + i8 * 16;
  const int la = i8 + 16 * ((lane >> 3) & 1);
  const unsigned moff = (lane < 16) ? (unsigned)(i8 * 16 + (lane >> 3) * 8) : OOB;
  u32x4 zx[4];
  auto zload = [&](int s) {
#pragma unroll
    for (int q = 0; q < 4; q++) zx[q] = ld_sc1(ws, OFF_Z + (((s * 80 + j) * 32 + 8 * w + 2 * q + half) * 5 + 1 + g) * 128 + i8 * 16);
  };
  drain(); TL(3, j == 0);
  const unsigned vfoff = lane == 0 ? (unsigned)(OFF_REC + 0) : OOB, vftgt = lane == 0 ? 32u : 0u;
  wait_flags(ws, vfoff, vftgt);
  zload(0);
  drain(); TL(4, j == 0);
  int Dm = p.dVm, De = p.dVe, Dl = p.dVl;
  unsigned tpub = now32();
  for (int s = 0; s < p.nsteps; s++) {
    STAMP_DECL STAMP_BEGIN STAMP(4 + w, 0);
    if (s > 0) wait_until(tpub + Dm);
    bool failm = gather_mem(ws, s, ml, w, lane);
    if (s > 0) adapt(Dm, failm, p.inc, p.dec);
    STAMP(4 + w, 1);
    __syncthreads();
    unsigned tm = now32();
    STAMP(4 + w, 2);
    f32x4 acc[4];
    MMState st; mm_pre_a(st, ml, aoff);
    mm_phase<4, 0, 4, false, true>(acc, st, B, ml, aoff, smem, idx, lane, NoFill());
#pragma unroll
    for (int pr = 0; pr < 2; pr++) {
      f32x4 o = halfsum2(acc[2 * pr], acc[2 * pr + 1]);
      int t = 2 * pr + (lane >> 5);
      int hl = 2 * t + ((lane >> 3) & 1);
      u32x2 v = {pk2(o[0], o[1]), pk2(o[2], o[3])};
      *(LAS u32x2*)(yl + w * 1024 + (hl * 8 + i8) * 16 + ((lane >> 4) & 1) * 8) = v;
    }
    bf16x8 yf = *(LAS bf16x8*)(yl + w * 1024 + ((4 * half + g) * 8 + i8) * 16);
    {
      u32x4 yv = *(LAS u32x4*)(yl + w * 1024 + lane * 16);
      st_sc1(ws, OFF_Z + (((s * 80 + j) * 32 + 8 * w + (lane >> 3)) * 5) * 128 + i8 * 16, yv);
    }
    STAMP(4 + w, 3);
    f32x4 ox[4], tot;
    {
      u32x4 ev[4]; unsigned al[4]; u32x4 pm;
      auto ld_e = [&]() {
#pragma unroll
        for (int q = 0; q < 4; q++) ev[q] = ld_sc1(ws, OFF_EBUF + (s * 32 + 8 * w + 2 * q + half) * 512 + g * 128 + i8 * 16);
      };
      auto ld_l = [&]() {
#pragma unroll
        for (int q = 0; q < 4; q++) al[q] = ld1_sc1(ws, OFF_ALB + (s * 32 + 8 * w + 2 * q + (g >> 1)) * 32 + i8 * 4);
        pm = ld_sc1(ws, OFF_PMB + (s * 32 + 8 * w + 4 * half + g) * 128 + i8 * 16);
      };
      wait_until(tm + De);
      ld_e();
      SB();
      wait_until(tm + Dl);
      ld_l();
      SB();
      unsigned spins = 0;
      while (true) {
        unsigned m = max(max(mx4(ev[0]), mx4(ev[1])), max(mx4(ev[2]), mx4(ev[3])));
        if (__all(m != SENT) || spins > SPIN_CAP) break;
        ++spins;
        asm volatile("" ::: "memory");
        ld_e();
        SB();
      }
      adapt(De, spins != 0, p.inc, p.dec);
      asm volatile("s_nop 1\n\tv_mfma_f32_16x16x32_bf16 %0, %4, %8, 0\n\tv_mfma_f32_16x16x32_bf16 %1, %5, %9, 0\n\tv_mfma_f32_16x16x32_bf16 %2, %6, %10, 0\n\tv_mfma_f32_16x16x32_bf16 %3, %7, %11, 0"
                   : "=&v"(ox[0]), "=&v"(ox[1]), "=&v"(ox[2]), "=&v"(ox[3]) : "v"(zx[0]), "v"(zx[1]), "v"(zx[2]), "v"(zx[3]), "v"(ev[0]), "v"(ev[1]), "v"(ev[2]), "v"(ev[3]));
      STAMP(4 + w, 4);
      spins = 0;
      while (true) {
        unsigned m = max(max(max(al[0], al[1]), max(al[2], al[3])), mx4(pm));
        if (__all(m != SENT) || spins > SPIN_CAP) break;
        ++spins;
        asm volatile("" ::: "memory");
        ld_l();
        SB();
      }
      adapt(Dl, spins != 0, p.inc, p.dec);
      f32x4 tm4;
      asm volatile("s_nop 1\n\tv_mfma_f32_16x16x32_bf16 %0, %1, %2, 0" : "=&v"(tm4) : "v"(yf), "v"(pm));
      asm volatile("s_nop 3" : "+v"(ox[0]), "+v"(ox[1]), "+v"(ox[2]), "+v"(ox[3]));
      tot = ox[0] * __uint_as_float(al[0]);
#pragma unroll
      for (int q = 1; q < 4; q++) tot += ox[q] * __uint_as_float(al[q]);
      asm volatile("s_nop 1" : "+v"(tm4), "+v"(tot));
      tot += tm4;
    }
    STAMP(4 + w, 5);
    *(LAS f32x4*)(red + w * 1024 + lane * 16) = tot;
    __syncthreads();
    if (w == 1) __builtin_amdgcn_raw_buffer_store_b32(1u, ws, lane == 0 ? (unsigned)(j * 4) : OOB, OFF_YF + s * 512, 16);
    STAMP(4 + w, 6);
    if (w == 0) {
      f32x4 r8[8];
#pragma unroll
      for (int ww = 0; ww < 4; ww++) { r8[2 * ww] = *(LAS f32x4*)(red + ww * 1024 + la * 16); r8[2 * ww + 1] = *(LAS f32x4*)(red + ww * 1024 + (la + 40) * 16); }
      SB();
      f32x4 sum = ((r8[0] + r8[1]) + (r8[2] + r8[3])) + ((r8[4] + r8[5]) + (r8[6] + r8[7]));
      u32x2 v = {san(pk2(sum[0], sum[1])), san(pk2(sum[2], sum[3]))};
      __builtin_amdgcn_raw_buffer_store_b64(v, ws, moff, OFF_MEMB + (s + 1) * 10240 + j * 128, 16);
    }
    tpub = now32();
    STAMP(4 + w, 7);
    if (s + 1 < NSEG) { wait_flags(ws, vfoff == OOB ? OOB : vfoff + (s + 1) * 128, vftgt); zload(s + 1); }
    STAMP_FLUSH(4 + w);
    if (lane == 0 && s == p.nsteps - 1) { unsigned* d = (unsigned*)(p.ws + OFF_TS + 32768 + (j * 4 + w) * 16); d[0] = Dm; d[1] = De; d[2] = Dl; }
  }
  __builtin_amdgcn_endpgm(); __builtin_unreachable();
}

template <bool AG> DEVI void mf0(f32x4& acc, const bf16x8& a, const bf16x8& b) {
  if constexpr (AG) asm volatile("v_mfma_f32_16x16x32_bf16 %0, %1, %2, 0" : "=&v"(acc) : "a"(a), "v"(b));
  else asm volatile("v_mfma_f32_16x16x32_bf16 %0, %1, %2, 0" : "=&v"(acc) : "v"(a), "v"(b));
}
template <bool AG> DEVI void mfa(f32x4& acc, const bf16x8& a, const bf16x8& b) {
  if constexpr (AG) asm volatile("v_mfma_f32_16x16x32_bf16 %0, %1, %2, %0" : "+v"(acc) : "a"(a), "v"(b));
  else asm volatile("v_mfma_f32_16x16x32_bf16 %0, %1, %2, %0" : "+v"(acc) : "v"(a), "v"(b));
}
template <bool AG> DEVI void mf0B(f32x4& acc, const bf16x8& a, const bf16x8& b) {
  if constexpr (AG) asm volatile("v_mfma_f32_16x16x32_bf16 %0, %1, %2, 0" : "=&v"(acc) : "v"(a), "a"(b));
  else asm volatile("v_mfma_f32_16x16x32_bf16 %0, %1, %2, 0" : "=&v"(acc) : "v"(a), "v"(b));
}
template <bool AG> DEVI void mfaB(f32x4& acc, const bf16x8& a, const bf16x8& b) {
  if constexpr (AG) asm volatile("v_mfma_f32_16x16x32_bf16 %0, %1, %2, %0" : "+v"(acc) : "v"(a), "a"(b));
  else asm volatile("v_mfma_f32_16x16x32_bf16 %0, %1, %2, %0" : "+v"(acc) : "v"(a), "v"(b));
}
DEVI bf16x8 tr8(LAS char* tile, int ldb, int row0, int colbyte, int lane) {
  const int g = lane >> 4, q4 = (lane & 15) >> 2;
  bf16x4 a = trread(tile + (row0 + 8 * g + q4) * ldb + colbyte), b = trread(tile + (row0 + 8 * g + 4 + q4) * ldb + colbyte);
  return __builtin_shufflevector(a, b, 0, 1, 2, 3, 4, 5, 6, 7);
}
DEVI u32x4 pack8(const f32x4& a, const f32x4& b) { return (u32x4){pk2(a[0], a[1]), pk2(a[2], a[3]), pk2(b[0], b[1]), pk2(b[2], b[3])}; }
DEVI float bf2f(unsigned short v) { return __uint_as_float((unsigned)v << 16); }
DEVI float rbf(float v) { return (float)(__bf16)v; }

DEVI void x_dma(const Params& p, int s, unsigned ldsbase, int w, int lane) {
  const char* src = p.ws + OFF_XNF + s * 40960 + w * 10240 + lane * 16;
#pragma unroll
  for (int i = 0; i < 10; i++) glds16(src + i * 1024, ldsbase + w * 10240 + i * 1024);
}

template <int NT, int NKS, class F>
DEVI void dense_phase(f32x4 (&acc)[NT][2], const bf16x8 (&W)[NT][NKS], LAS char* xb, int lane, F filler) {
  bf16x8 b[3][2];
#pragma unroll
  for (int q = 0; q < 2; q++) { b[q][0] = *(LAS bf16x8*)(xb + (q * 2) * 1024 + lane * 16); b[q][1] = *(LAS bf16x8*)(xb + (q * 2 + 1) * 1024 + lane * 16); }
#pragma unroll
  for (int kk = 0; kk < NKS; kk++) {
    if (kk + 2 < NKS) { b[(kk + 2) % 3][0] = *(LAS bf16x8*)(xb + ((kk + 2) * 2) * 1024 + lane * 16); b[(kk + 2) % 3][1] = *(LAS bf16x8*)(xb + ((kk + 2) * 2 + 1) * 1024 + lane * 16); }
    SB();
#pragma unroll
    for (int t = 0; t < NT; t++)
#pragma unroll
      for (int tt = 0; tt < 2; tt++) {
        if (kk == 0) { if (t * NKS + kk < 64) mf0<true>(acc[t][tt], W[t][kk], b[kk % 3][tt]); else mf0<false>(acc[t][tt], W[t][kk], b[kk % 3][tt]); }
        else { if (t * NKS + kk < 64) mfa<true>(acc[t][tt], W[t][kk], b[kk % 3][tt]); else mfa<false>(acc[t][tt], W[t][kk], b[kk % 3][tt]); }
      }
    filler(kk);
    SB();
  }
  asm volatile("s_nop 7" ::: "memory");
#pragma unroll
  for (int t = 0; t < NT; t++) asm volatile("" : "+v"(acc[t][0]), "+v"(acc[t][1]));
}

DEVI void role_QKV(const Params& p, int c, LAS char* smem, rsrc_t ws) {
  int tid = threadIdx.x; asm volatile("; roleQKV" : "+v"(tid) :: "memory");
  const int lane = tid & 63, w = __builtin_amdgcn_readfirstlane(tid >> 6);
  const unsigned lds0 = (unsigned)(size_t)smem;
  bf16x8 W[5][20];
  x_dma(p, 0, lds0 + 81920, w, lane);
  {
    const int pc = tid % 40, r0 = tid / 40;
    const int gcol = 320 * c + 8 * pc;
    const bool ok = tid < 240 && gcol < 18432;
    const int mat = gcol / 6144, cm = gcol - mat * 6144;
    const uint16_t* base = (mat == 0 ? p.Wq : mat == 1 ? p.Wk : p.Wv) + cm;
    u32x4 pre[11];
    auto issue = [&](int ch) {
#pragma unroll
      for (int i = 0; i < 11; i++) { int r = r0 + 6 * i; pre[i] = (ok && r < 64) ? *(const u32x4*)(base + (size_t)(64 * ch + r) * 6144) : (u32x4){0, 0, 0, 0}; }
    };
    issue(0);
    const int p4 = lane & 3;
#pragma unroll
    for (int ch = 0; ch < 10; ch++) {
#pragma unroll
      for (int i = 0; i < 11; i++) { int r = r0 + 6 * i; if (tid < 240 && r < 64) *(LAS u32x4*)(smem + r * 640 + pc * 16) = pre[i]; }
      __syncthreads();
      if (ch < 9) issue(ch + 1);
#pragma unroll
      for (int k2 = 0; k2 < 2; k2++)
#pragma unroll
        for (int t = 0; t < 5; t++) {
          const int blk = t < 4 ? 2 * w + (t >> 1) : 8 + (w >> 1), u = t < 4 ? (t & 1) : (w & 1);
          W[t][2 * ch + k2] = tr8(smem, 640, 32 * k2, (32 * blk + 8 * p4 + 4 * u) * 2, lane);
        }
      __syncthreads();
    }
  }
  const int nb = min(10, 576 - 10 * c);
  unsigned dst[3];
#pragma unroll
  for (int q = 0; q < 3; q++) {
    int blk = q < 2 ? 2 * w + q : 8 + (w >> 1);
    int gb = 10 * c + blk; int mat = gb / 192, bb = gb - mat * 192;
    dst[q] = blk < nb ? OFF_QF + mat * SZ_QF + bb * 2048 + lane * 16 + (q == 2 ? (w & 1) * 8 : 0) : OOB;
  }
  const int g0 = (10 * c) / 6;
  const int gid = g0 + lane; const int lo = max(10 * c, 6 * gid), hi = min(10 * c + nb, 6 * gid + 6);
  drain(); __syncthreads();
  TL(5, c == 0);
  for (int s = 0; s < NSEG; s++) {
    LAS char* xb = smem + (((s + 1) & 1) ? 81920 : 0);
    const int sn = s + 1 < NSEG ? s + 1 : s;
    const char* src = p.ws + OFF_XNF + sn * 40960 + w * 10240 + lane * 16;
    const unsigned ldst = lds0 + ((s & 1) ? 81920 : 0) + w * 10240;
    f32x4 acc[5][2];
    GST(0, s, 0, c == 0);
    dense_phase<5, 20>(acc, W, xb, lane, [&](int kk) { if (kk < 10) glds16(src + kk * 1024, ldst + kk * 1024); });
    GST(0, s, 1, c == 0);
    drain(); __syncthreads();
    GST(0, s, 2, c == 0);
    if (s > 2 && w == 0 && lane < 3 && hi > lo) atom_add(p.ws, ((s - 1) * 128 + gid) * 4, hi - lo);
#pragma unroll
    for (int tt = 0; tt < 2; tt++) {
      const unsigned so = s * 192 * 2048 + tt * 1024;
      __builtin_amdgcn_raw_buffer_store_b128(pack8(acc[0][tt], acc[1][tt]), ws, dst[0], so, 16);
      __builtin_amdgcn_raw_buffer_store_b128(pack8(acc[2][tt], acc[3][tt]), ws, dst[1], so, 16);
      __builtin_amdgcn_raw_buffer_store_b64((u32x2){pk2(acc[4][tt][0], acc[4][tt][1]), pk2(acc[4][tt][2], acc[4][tt][3])}, ws, dst[2], so, 16);
    }
    if (s < 2) { drain(); __syncthreads(); if (w == 0 && lane < 3 && hi > lo) atom_add(p.ws, (s * 128 + gid) * 4, hi - lo); }
  }
  drain(); __syncthreads();
  if (w == 0 && lane < 3 && hi > lo) atom_add(p.ws, ((NSEG - 1) * 128 + gid) * 4, hi - lo);
  TL(6, c == 0);
  __builtin_amdgcn_endpgm(); __builtin_unreachable();
}

DEVI void role_VO(const Params& p, int h, LAS char* smem, rsrc_t ws) {
  int tid = threadIdx.x; asm volatile("; roleVO" : "+v"(tid) :: "memory");
  const int lane = tid & 63, w = __builtin_amdgcn_readfirstlane(tid >> 6);
  bf16x8 W[10][6];
  {
    rsrc_t ro = __builtin_amdgcn_make_buffer_rsrc((void*)(p.Wo + (size_t)h * 192 * 640), 0, 192 * 640 * 2, 0x00020000);
    u32x4 pre[10];
    auto issue = [&](int ch) {
#pragma unroll
      for (int i = 0; i < 10; i++) pre[i] = __builtin_amdgcn_raw_buffer_load_b128(ro, (i * 256 + tid) * 16, ch * 40960, 0);
    };
    issue(0);
    const int p4 = lane & 3;
#pragma unroll
    for (int ch = 0; ch < 6; ch++) {
#pragma unroll
      for (int i = 0; i < 10; i++) *(LAS u32x4*)(smem + (i * 256 + tid) * 16) = pre[i];
      __syncthreads();
      if (ch < 5) issue(ch + 1);
#pragma unroll
      for (int t = 0; t < 10; t++) W[t][ch] = tr8(smem, 1280, 0, (16 * (10 * w + t) + 4 * p4) * 2, lane);
      __syncthreads();
    }
  }
  const int n = lane & 15, g = lane >> 4;
  const unsigned foff = lane == 0 ? (unsigned)((64 + h) * 4) : OOB;
  const unsigned ftgt = lane == 0 ? 6u : 0u;
  const unsigned zl = ((((20 * w + (n >> 3)) * 32 + h) * 5 + 1 + g) * 128 + (n & 7) * 16);
  bf16x8 a[2][2][6];
  auto aload = [&](int s, bf16x8 (&d)[2][6]) {
#pragma unroll
    for (int ks = 0; ks < 6; ks++)
#pragma unroll
      for (int mt = 0; mt < 2; mt++) d[mt][ks] = __builtin_bit_cast(bf16x8, __builtin_amdgcn_raw_buffer_load_b128(ws, lane * 16, OFF_VF + ((s * 192 + h * 6 + ks) * 2 + mt) * 1024, 16));
  };
  auto half = [&](f32x4 (&acc)[5][2], const bf16x8 (&d)[2][6], auto HF) {
    constexpr int hf = decltype(HF)::value;
#pragma unroll
    for (int ks = 0; ks < 6; ks++)
#pragma unroll
      for (int t = 0; t < 5; t++)
#pragma unroll
        for (int mt = 0; mt < 2; mt++) {
          if (ks == 0) mf0B<true>(acc[t][mt], d[mt][ks], W[5 * hf + t][ks]); else mfaB<true>(acc[t][mt], d[mt][ks], W[5 * hf + t][ks]);
        }
  };
  auto store = [&](f32x4 (&acc)[5][2], int s, int hf) {
#pragma unroll
    for (int t = 0; t < 5; t++) {
      asm volatile("" : "+v"(acc[t][0]), "+v"(acc[t][1]));
      __builtin_amdgcn_raw_buffer_store_b128(pack8(acc[t][0], acc[t][1]), ws, zl + (5 * hf + t) * 2 * 20480, OFF_Z + s * 80 * 20480, 16);
    }
  };
  auto step = [&](int s, bf16x8 (&cur)[2][6], bf16x8 (&nxt)[2][6]) {
    const int sn = s + 1 < NSEG ? s + 1 : s;
    const unsigned fo = foff == OOB ? OOB : foff + sn * 512;
    unsigned fl = ld1_sc1(ws, fo);
    SB();
    f32x4 acc0[5][2], acc1[5][2];
    GST(1, s, 0, h == 0);
    half(acc0, cur, std::integral_constant<int, 0>());
    SB();
    GST(1, s, 1, h == 0);
    if (!__all(fl >= ftgt)) wait_flags(ws, fo, ftgt);
    drain(); __syncthreads();
    GST(1, s, 2, h == 0);
    if (s > 1 && tid == 0) atom_add(p.ws, OFF_REC + (s - 1) * 128 + 0, 1);
    aload(sn, nxt);
    store(acc0, s, 0);
    SB();
    GST(1, s, 3, h == 0);
    half(acc1, cur, std::integral_constant<int, 1>());
    asm volatile("s_nop 7" ::: "memory");
    GST(1, s, 4, h == 0);
    store(acc1, s, 1);
    if (s < 1) { drain(); __syncthreads(); if (tid == 0) atom_add(p.ws, OFF_REC + s * 128 + 0, 1); }
  };
  TL(7, h == 0);
  wait_flags(ws, foff, ftgt);
  TL(8, h == 0);
  aload(0, a[0]);
  for (int s = 0; s < NSEG; s += 2) { step(s, a[0], a[1]); step(s + 1, a[1], a[0]); }
  drain(); __syncthreads();
  if (tid == 0) atom_add(p.ws, OFF_REC + (NSEG - 1) * 128 + 0, 1);
  TL(9, h == 0);
  __builtin_amdgcn_endpgm(); __builtin_unreachable();
}

DEVI void role_GU(const Params& p, int c, LAS char* smem, rsrc_t ws) {
  int tid = threadIdx.x; asm volatile("; roleGU" : "+v"(tid) :: "memory");
  const int lane = tid & 63, w = __builtin_amdgcn_readfirstlane(tid >> 6);
  const unsigned lds0 = (unsigned)(size_t)smem;
  bf16x8 W[4][20];
  x_dma(p, 0, lds0 + 81920, w, lane);
  {
    rsrc_t rg = __builtin_amdgcn_make_buffer_rsrc((void*)p.Wg, 0, 640 * 1536 * 2, 0x00020000);
    rsrc_t ru = __builtin_amdgcn_make_buffer_rsrc((void*)p.Wu, 0, 640 * 1536 * 2, 0x00020000);
    u32x4 pre[8];
    auto issue = [&](int ch) {
#pragma unroll
      for (int i = 0; i < 8; i++) { int u = i * 256 + tid; int row = (u >> 4) & 63, pc = u & 15;
        pre[i] = __builtin_amdgcn_raw_buffer_load_b128(i < 4 ? rg : ru, row * 3072 + pc * 16, ch * 64 * 3072 + c * 256, 0); }
    };
    issue(0);
    const int p4 = lane & 3;
#pragma unroll
    for (int ch = 0; ch < 10; ch++) {
#pragma unroll
      for (int i = 0; i < 8; i++) *(LAS u32x4*)(smem + (i * 256 + tid) * 16) = pre[i];
      __syncthreads();
      if (ch < 9) issue(ch + 1);
#pragma unroll
      for (int k2 = 0; k2 < 2; k2++)
#pragma unroll
        for (int t = 0; t < 4; t++) W[t][2 * ch + k2] = tr8(smem + (t >> 1) * 16384, 256, 32 * k2, (32 * w + 8 * p4 + 4 * (t & 1)) * 2, lane);
      __syncthreads();
    }
  }
  drain(); __syncthreads();
  for (int s = 0; s < NSEG; s++) {
    LAS char* xb = smem + (((s + 1) & 1) ? 81920 : 0);
    const int sn = s + 1 < NSEG ? s + 1 : s;
    const char* src = p.ws + OFF_XNF + sn * 40960 + w * 10240 + lane * 16;
    const unsigned ldst = lds0 + ((s & 1) ? 81920 : 0) + w * 10240;
    f32x4 acc[4][2];
    dense_phase<4, 20>(acc, W, xb, lane, [&](int kk) { if (kk < 10) glds16(src + kk * 1024, ldst + kk * 1024); });
    drain(); __syncthreads();
    if (s > 0 && tid == 0) atom_add(p.ws, OFF_REC + (s - 1) * 128 + 4, 1);
#pragma unroll
    for (int tt = 0; tt < 2; tt++) {
      f32x4 h0, h1;
#pragma unroll
      for (int r = 0; r < 4; r++) { h0[r] = rbf(acc[0][tt][r]) * rbf(acc[2][tt][r]); h1[r] = rbf(acc[1][tt][r]) * rbf(acc[3][tt][r]); }
      __builtin_amdgcn_raw_buffer_store_b128(pack8(h0, h1), ws, lane * 16, OFF_HF + ((s * 48 + 4 * c + w) * 2 + tt) * 1024, 16);
    }
  }
  drain(); __syncthreads();
  if (tid == 0) atom_add(p.ws, OFF_REC + (NSEG - 1) * 128 + 4, 1);
  __builtin_amdgcn_endpgm(); __builtin_unreachable();
}

DEVI void role_DN(const Params& p, int c, LAS char* smem, rsrc_t ws) {
  int tid = threadIdx.x; asm volatile("; roleDN" : "+v"(tid) :: "memory");
  const int lane = tid & 63, w = __builtin_amdgcn_readfirstlane(tid >> 6);
  bf16x8 W[2][48];
  {
    rsrc_t rd = __builtin_amdgcn_make_buffer_rsrc((void*)p.Wd, 0, 1536 * 640 * 2, 0x00020000);
    u32x4 pre[8];
    auto issue = [&](int ch) {
#pragma unroll
      for (int i = 0; i < 8; i++) { int u = i * 256 + tid; int row = u >> 4, pc = u & 15;
        pre[i] = __builtin_amdgcn_raw_buffer_load_b128(rd, row * 1280 + pc * 16, ch * 128 * 1280 + c * 256, 0); }
    };
    issue(0);
    const int p4 = lane & 3;
#pragma unroll
    for (int ch = 0; ch < 12; ch++) {
#pragma unroll
      for (int i = 0; i < 8; i++) *(LAS u32x4*)(smem + (i * 256 + tid) * 16) = pre[i];
      __syncthreads();
      if (ch < 11) issue(ch + 1);
#pragma unroll
      for (int k4 = 0; k4 < 4; k4++)
#pragma unroll
        for (int t = 0; t < 2; t++) W[t][4 * ch + k4] = tr8(smem, 256, 32 * k4, (32 * w + 8 * p4 + 4 * t) * 2, lane);
      __syncthreads();
    }
  }
  const unsigned foff = lane == 0 ? (unsigned)(OFF_REC + 4) : OOB;
  const unsigned ftgt = lane == 0 ? 12u : 0u;
  const int n = lane & 15, g = lane >> 4;
  u32x4 st[12];
  auto hload = [&](int s, int hf) {
#pragma unroll
    for (int i = 0; i < 12; i++) st[i] = __builtin_amdgcn_raw_buffer_load_b128(ws, (i * 256 + tid) * 16, OFF_HF + s * 98304 + hf * 49152, 16);
  };
  wait_flags(ws, foff, ftgt);
  hload(0, 0);
  for (int s = 0; s < NSEG; s++) {
    f32x4 acc[2][2];
#pragma unroll
    for (int hf = 0; hf < 2; hf++) {
#pragma unroll
      for (int i = 0; i < 12; i++) *(LAS u32x4*)(smem + hf * 49152 + (i * 256 + tid) * 16) = st[i];
      GST(2, s, 3 * hf, c == 0);
      if (hf == 0) drain();
      __syncthreads();
      GST(2, s, 3 * hf + 1, c == 0);
      if (hf == 0) { if (s > 0 && tid == 0) atom_add(p.ws, OFF_REC + (s - 1) * 128 + 8, 1); hload(s, 1); }
      else if (s + 1 < NSEG) { wait_flags(ws, foff == OOB ? OOB : foff + (s + 1) * 128, ftgt); hload(s + 1, 0); }
      GST(2, s, 3 * hf + 2, c == 0);
      LAS char* xb = smem + hf * 49152;
      bf16x8 b[3][2];
#pragma unroll
      for (int q = 0; q < 2; q++) { b[q][0] = *(LAS bf16x8*)(xb + (q * 2) * 1024 + lane * 16); b[q][1] = *(LAS bf16x8*)(xb + (q * 2 + 1) * 1024 + lane * 16); }
#pragma unroll
      for (int kk = 0; kk < 24; kk++) {
        if (kk + 2 < 24) { b[(kk + 2) % 3][0] = *(LAS bf16x8*)(xb + ((kk + 2) * 2) * 1024 + lane * 16); b[(kk + 2) % 3][1] = *(LAS bf16x8*)(xb + ((kk + 2) * 2 + 1) * 1024 + lane * 16); }
        SB();
        const int kg = hf * 24 + kk;
#pragma unroll
        for (int t = 0; t < 2; t++)
#pragma unroll
          for (int tt = 0; tt < 2; tt++) {
            if (kg == 0) mf0<true>(acc[t][tt], W[t][kg], b[kk % 3][tt]);
            else if (t * 48 + kg < 64) mfa<true>(acc[t][tt], W[t][kg], b[kk % 3][tt]);
            else mfa<false>(acc[t][tt], W[t][kg], b[kk % 3][tt]);
          }
        SB();
      }
    }
    asm volatile("s_nop 7" ::: "memory");
#pragma unroll
    for (int tt = 0; tt < 2; tt++) {
      asm volatile("" : "+v"(acc[0][tt]), "+v"(acc[1][tt]));
      f32x4 g0, g1;
#pragma unroll
      for (int r = 0; r < 4; r++) {
        float d0 = rbf(acc[0][tt][r]), d1 = rbf(acc[1][tt][r]);
        g0[r] = d0 / (1.0f + __expf(-d0)); g1[r] = d1 / (1.0f + __expf(-d1));
      }
      const int tok = 32 * s + 8 * (n >> 2) + 4 * tt + (n & 3);
      __builtin_amdgcn_raw_buffer_store_b128(pack8(g0, g1), ws, OFF_G + tok * 1280 + (128 * c + 32 * w + 8 * g) * 2, 0, 16);
    }
  }
  drain(); __syncthreads();
  if (tid == 0) atom_add(p.ws, OFF_REC + (NSEG - 1) * 128 + 8, 1);
  TL(10, c == 0);
  __builtin_amdgcn_endpgm(); __builtin_unreachable();
}

DEVI void token_A(const Params& p, int s, int h, rsrc_t ws, int lane) {
  const int n = lane & 15, g = lane >> 4;
  {
    unsigned off = lane == 0 ? (unsigned)((s * 128 + h) * 4) : lane == 1 ? (unsigned)((s * 128 + 32 + h) * 4) : lane == 2 ? (unsigned)((s * 128 + 96 + h) * 4) : OOB;
    unsigned tgt = lane < 2 ? 6u : lane == 2 ? 1u : 0u;
    wait_flags(ws, off, tgt);
  }
  bf16x8 qf[2][6], kf[2][6], km[6];
#pragma unroll
  for (int ks = 0; ks < 6; ks++) {
#pragma unroll
    for (int tt = 0; tt < 2; tt++) {
      qf[tt][ks] = __builtin_bit_cast(bf16x8, __builtin_amdgcn_raw_buffer_load_b128(ws, lane * 16, OFF_QF + ((s * 192 + h * 6 + ks) * 2 + tt) * 1024, 16));
      kf[tt][ks] = __builtin_bit_cast(bf16x8, __builtin_amdgcn_raw_buffer_load_b128(ws, lane * 16, OFF_KF + ((s * 192 + h * 6 + ks) * 2 + tt) * 1024, 16));
    }
    km[ks] = __builtin_bit_cast(bf16x8, __builtin_amdgcn_raw_buffer_load_b128(ws, g * 128 + (n & 7) * 16, OFF_KM + ((s * 32 + h) * 6 + ks) * 512, 16));
  }
  const float cs = 0.07216878364870322f * 1.4426950408889634f;
#pragma unroll
  for (int tq = 0; tq < 2; tq++) {
    f32x4 T0 = {0, 0, 0, 0}, T1 = {0, 0, 0, 0}, T2 = {0, 0, 0, 0};
#pragma unroll
    for (int ks = 0; ks < 6; ks++) {
      T0 = __builtin_amdgcn_mfma_f32_16x16x32_bf16(km[ks], qf[tq][ks], T0, 0, 0, 0);
      T1 = __builtin_amdgcn_mfma_f32_16x16x32_bf16(kf[0][ks], qf[tq][ks], T1, 0, 0, 0);
      T2 = __builtin_amdgcn_mfma_f32_16x16x32_bf16(kf[1][ks], qf[tq][ks], T2, 0, 0, 0);
    }
    const int qt = 8 * (n >> 2) + 4 * tq + (n & 3);
    float m = fmaxf(fmaxf(T0[0], T0[1]), fmaxf(T0[2], T0[3]));
#pragma unroll
    for (int r = 0; r < 4; r++) {
      if (8 * g + r > qt) T1[r] = -3.0e38f;
      if (8 * g + 4 + r > qt) T2[r] = -3.0e38f;
      m = fmaxf(m, fmaxf(T1[r], T2[r]));
    }
    m = xor32max(xor16max(m));
    const float mc = m * cs;
    float l0 = 0, lx = 0; f32x4 E0, E1, E2;
#pragma unroll
    for (int r = 0; r < 4; r++) {
      E0[r] = __builtin_amdgcn_exp2f(__builtin_fmaf(T0[r], cs, -mc));
      E1[r] = (8 * g + r > qt) ? 0.f : __builtin_amdgcn_exp2f(__builtin_fmaf(T1[r], cs, -mc));
      E2[r] = (8 * g + 4 + r > qt) ? 0.f : __builtin_amdgcn_exp2f(__builtin_fmaf(T2[r], cs, -mc));
      l0 += E0[r]; lx += E1[r] + E2[r];
    }
    const float inv = 1.0f / (xor16add(l0) + xor32add(xor16add(lx)));
    E0 *= inv; E1 *= inv; E2 *= inv;
    const unsigned so = OFF_PX + (s * 160 + h * 5) * 512 + tq * 256;
    __builtin_amdgcn_raw_buffer_store_b64((u32x2){pk2(E0[0], E0[1]), pk2(E0[2], E0[3])}, ws, g < 2 ? (unsigned)(n * 16 + g * 8) : OOB, so, 16);
    __builtin_amdgcn_raw_buffer_store_b128(pack8(E1, E2), ws, (1 + g) * 512 + n * 16, so, 16);
  }
  drain();
  if (lane == 0) atom_add(p.ws, OFF_REC + s * 128 + 16, 1);
}

DEVI void token_B(const Params& p, int s, int cg, LAS char* smem, rsrc_t ws, int tid, int lane, int w) {
  if (w == 0) {
    unsigned off = lane < 3 ? (unsigned)(OFF_REC + s * 128 + (lane == 0 ? 0 : lane == 1 ? 8 : 16)) : OOB;
    unsigned tgt = lane == 0 ? 32u : lane == 1 ? 5u : lane == 2 ? 32u : 0u;
    wait_flags(ws, off, tgt);
    wait_flags(ws, OFF_YF + s * 512 + lane * 4, 1u);
    wait_flags(ws, lane < 16 ? (unsigned)(OFF_YF + s * 512 + 256 + lane * 4) : OOB, lane < 16 ? 1u : 0u);
  }
  __syncthreads();
#pragma unroll
  for (int hf = 0; hf < 2; hf++) {
    u32x4 st[10];
#pragma unroll
    for (int i = 0; i < 10; i++) st[i] = __builtin_amdgcn_raw_buffer_load_b128(ws, (i * 256 + tid) * 16, OFF_PX + s * 81920 + hf * 40960, 16);
#pragma unroll
    for (int i = 0; i < 10; i++) *(LAS u32x4*)(smem + hf * 40960 + (i * 256 + tid) * 16) = st[i];
  }
  const int n = lane & 15, g = lane >> 4;
  const unsigned zoff = ((16 * cg + 4 * w + (n >> 3)) * 160 + g) * 128 + (n & 7) * 16;
  const unsigned zs = OFF_Z + s * 80 * 20480;
  constexpr int ZP = 8;
  bf16x8 z[ZP][2];
#pragma unroll
  for (int q = 0; q < ZP; q++)
#pragma unroll
    for (int u = 0; u < 2; u++) z[q][u] = __builtin_bit_cast(bf16x8, __builtin_amdgcn_raw_buffer_load_b128(ws, zoff + u * 2 * 20480 + q * 512, zs, 16));
  __syncthreads();
  f32x4 acc[2][2] = {{{0, 0, 0, 0}, {0, 0, 0, 0}}, {{0, 0, 0, 0}, {0, 0, 0, 0}}};
#pragma unroll
  for (int ks = 0; ks < 40; ks++) {
    bf16x8 b0 = *(LAS bf16x8*)(smem + (4 * ks + g) * 512 + n * 16), b1 = *(LAS bf16x8*)(smem + (4 * ks + g) * 512 + 256 + n * 16);
    bf16x8 z0 = z[ks % ZP][0], z1 = z[ks % ZP][1];
    if (ks + ZP < 40) {
#pragma unroll
      for (int u = 0; u < 2; u++) z[ks % ZP][u] = __builtin_bit_cast(bf16x8, __builtin_amdgcn_raw_buffer_load_b128(ws, zoff + u * 2 * 20480 + (ks + ZP) * 512, zs, 16));
    }
    acc[0][0] = __builtin_amdgcn_mfma_f32_16x16x32_bf16(z0, b0, acc[0][0], 0, 0, 0);
    acc[0][1] = __builtin_amdgcn_mfma_f32_16x16x32_bf16(z0, b1, acc[0][1], 0, 0, 0);
    acc[1][0] = __builtin_amdgcn_mfma_f32_16x16x32_bf16(z1, b0, acc[1][0], 0, 0, 0);
    acc[1][1] = __builtin_amdgcn_mfma_f32_16x16x32_bf16(z1, b1, acc[1][1], 0, 0, 0);
  }
#pragma unroll
  for (int u = 0; u < 2; u++)
#pragma unroll
    for (int tq = 0; tq < 2; tq++) {
      const int tok = 32 * s + 8 * (n >> 2) + 4 * tq + (n & 3);
      const int col = 128 * cg + 32 * w + 16 * u + 4 * g;
      u32x2 xv = *(const u32x2*)(p.x + (size_t)tok * 640 + col);
      u32x2 gv = __builtin_amdgcn_raw_buffer_load_b64(ws, OFF_G + tok * 1280 + col * 2, 0, 16);
      float o[4];
#pragma unroll
      for (int r = 0; r < 4; r++) {
        unsigned xw = r < 2 ? xv.x : xv.y, gw = r < 2 ? gv.x : gv.y;
        float xf = __uint_as_float((r & 1) ? (xw & 0xffff0000u) : (xw << 16)), gf = __uint_as_float((r & 1) ? (gw & 0xffff0000u) : (gw << 16));
        o[r] = xf + rbf(rbf(acc[u][tq][r]) * gf);
      }
      *(u32x2*)(p.out + (size_t)tok * 640 + col) = (u32x2){pk2(o[0], o[1]), pk2(o[2], o[3])};
    }
  __syncthreads();
}

DEVI void role_TK(const Params& p, int t, LAS char* smem, rsrc_t ws) {
  int tid = threadIdx.x; asm volatile("; roleTK" : "+v"(tid) :: "memory");
  const int lane = tid & 63, w = __builtin_amdgcn_readfirstlane(tid >> 6);
  for (int idx = t; idx < NSEG * 13; idx += N_TK) {
    const int s = idx / 13, k = idx - s * 13;
    GST(3, (idx / N_TK), 0, t == 0);
    if (k < 8) token_A(p, s, 4 * k + w, ws, lane);
    else token_B(p, s, k - 8, smem, ws, tid, lane, w);
    GST(3, (idx / N_TK), 1, t == 0);
  }
  TL(16 + t, true);
  __builtin_amdgcn_endpgm(); __builtin_unreachable();
}

constexpr int PB_LN = 512, PB_FILL = 64, PB_B = 320;
__global__ __launch_bounds__(256) void k_pre(Params p) {
  __shared__ __attribute__((aligned(16))) char sm_[24576];
  LAS char* sm = (LAS char*)sm_;
  const int tid = threadIdx.x, lane = tid & 63, w = tid >> 6;
  int b = blockIdx.x;
  TL(11, b == 0);
  if (b < PB_B) {
    const int h = b / 10, nb = b % 10;
    for (int u = tid; u < 192 * 8; u += 256) { int row = u >> 3, pc = u & 7; *(LAS u32x4*)(sm + row * 128 + pc * 16) = *(const u32x4*)(p.Wo + ((size_t)h * 192 + row) * 640 + 64 * nb + pc * 8); }
    __syncthreads();
    bf16x8 Bf[4][6];
    const int p4 = lane & 3;
#pragma unroll
    for (int nt = 0; nt < 4; nt++)
#pragma unroll
      for (int ks = 0; ks < 6; ks++) Bf[nt][ks] = tr8(sm, 128, 32 * ks, (16 * nt + 4 * p4) * 2, lane);
    const int m = lane & 15, g = lane >> 4;
    for (int kk = w; kk < 10; kk += 4) {
      f32x4 acc[4][4];
#pragma unroll
      for (int mt = 0; mt < 4; mt++)
#pragma unroll
        for (int nt = 0; nt < 4; nt++) acc[mt][nt] = (f32x4){0, 0, 0, 0};
#pragma unroll
      for (int ks = 0; ks < 6; ks++)
#pragma unroll
        for (int mt = 0; mt < 4; mt++) {
          bf16x8 a = *(const bf16x8*)(p.Wv + (size_t)(64 * kk + 16 * mt + m) * 6144 + h * 192 + 32 * ks + 8 * g);
#pragma unroll
          for (int nt = 0; nt < 4; nt++) acc[mt][nt] = __builtin_amdgcn_mfma_f32_16x16x32_bf16(a, Bf[nt][ks], acc[mt][nt], 0, 0, 0);
        }
#pragma unroll
      for (int u = 0; u < 2; u++)
#pragma unroll
        for (int nt = 0; nt < 4; nt++) {
          const f32x4 &A = acc[u][nt], &B = acc[u + 2][nt];
          u32x4 v = {pk2(A[0], A[1]), pk2(B[0], B[1]), pk2(A[2], A[3]), pk2(B[2], B[3])};
          const int f = 64 * nb + 16 * nt + m;
          *(u32x4*)(p.ws + OFF_BF + (((f >> 3) * 16 + (h >> 1)) * 10 + kk) * 2048 + u * 1024 + ((h & 1) * 8 + (f & 7) + 16 * g) * 16) = v;
        }
    }
    return;
  }
  b -= PB_B;
  if (b < PB_LN) {
    const int tok = b * 4 + w;
    const uint16_t* xr = p.x + (size_t)tok * 640;
    u32x4 v0 = *(const u32x4*)(xr + lane * 8), v1 = {0, 0, 0, 0};
    if (lane < 16) v1 = *(const u32x4*)(xr + 512 + lane * 8);
    float f[16];
#pragma unroll
    for (int i = 0; i < 4; i++) {
      unsigned a = v0[i], c = v1[i];
      f[2 * i] = __uint_as_float(a << 16); f[2 * i + 1] = __uint_as_float(a & 0xffff0000u);
      f[8 + 2 * i] = __uint_as_float(c << 16); f[8 + 2 * i + 1] = __uint_as_float(c & 0xffff0000u);
    }
    float sum = 0;
#pragma unroll
    for (int i = 0; i < 16; i++) sum += f[i];
    for (int o = 32; o; o >>= 1) sum += __shfl_xor(sum, o);
    const float mean = sum * (1.0f / 640.0f);
    float sq = 0;
#pragma unroll
    for (int i = 0; i < 8; i++) { float d = f[i] - mean; sq += d * d; }
    if (lane < 16) {
#pragma unroll
      for (int i = 8; i < 16; i++) { float d = f[i] - mean; sq += d * d; }
    }
    for (int o = 32; o; o >>= 1) sq += __shfl_xor(sq, o);
    const float mb = rbf(mean), vb = rbf(sq * (1.0f / 640.0f));
    const float den = rbf(sqrtf(rbf(vb + 1e-5f)));
    float y[16];
#pragma unroll
    for (int i = 0; i < 16; i++) y[i] = rbf(f[i] - mb) / den;
    const int s = tok >> 5, t = tok & 31, tt = (t >> 2) & 1, n = 4 * (t >> 3) + (t & 3);
    {
      int pc = lane; int cb = pc >> 2, g = pc & 3;
      *(u32x4*)(p.ws + OFF_XNF + ((s * 20 + cb) * 2 + tt) * 1024 + (n + 16 * g) * 16) = (u32x4){pk2(y[0], y[1]), pk2(y[2], y[3]), pk2(y[4], y[5]), pk2(y[6], y[7])};
      if (lane < 16) { pc = 64 + lane; cb = pc >> 2; g = pc & 3;
        *(u32x4*)(p.ws + OFF_XNF + ((s * 20 + cb) * 2 + tt) * 1024 + (n + 16 * g) * 16) = (u32x4){pk2(y[8], y[9]), pk2(y[10], y[11]), pk2(y[12], y[13]), pk2(y[14], y[15])}; }
    }
    return;
  }
  b -= PB_LN;
  const u32x4 ones = {SENT, SENT, SENT, SENT}, zero = {0, 0, 0, 0};
  for (unsigned o = OFF_MEMB + 10240 + (b * 256 + tid) * 16; o < SENT_END; o += PB_FILL * 256 * 16) *(u32x4*)(p.ws + o) = ones;
  for (unsigned o = (b * 256 + tid) * 16; o < FLAGS_END; o += PB_FILL * 256 * 16) *(u32x4*)(p.ws + o) = zero;
  if (b == 0) {
    for (int u = tid; u < 640; u += 256) {
      int j = u >> 3, i = u & 7;
      u32x4 v = *(const u32x4*)(p.mem0 + i * 640 + j * 8);
      v.x = san(v.x); v.y = san(v.y); v.z = san(v.z); v.w = san(v.w);
      *(u32x4*)(p.ws + OFF_MEMB + j * 128 + i * 16) = v;
    }
  }
}

__global__ __launch_bounds__(256, 1) void k_main(Params p) {
  extern __shared__ __attribute__((aligned(16))) char smem_[];
  LAS char* smem = (LAS char*)smem_;
  rsrc_t ws = __builtin_amdgcn_make_buffer_rsrc(p.ws, 0, 0x7fffffff, 0x00020000);
  int b = blockIdx.x;
  TL(0, b == 0);
  if (b < N_S) { if (p.mask & 1) role_S(p, b, smem, ws); return; }
  b -= N_S;
  if (b < N_V) { if (p.mask & 2) role_V(p, b, smem, ws); return; }
  b -= N_V;
  if (b < N_QKV) { if (p.mask & 4) role_QKV(p, b, smem, ws); return; }
  b -= N_QKV;
  if (b < N_VO) { if (p.mask & 8) role_VO(p, b, smem, ws); return; }
  b -= N_VO;
  if (b < N_GU) { if (p.mask & 16) role_GU(p, b, smem, ws); return; }
  b -= N_GU;
  if (b < N_DN) { if (p.mask & 32) role_DN(p, b, smem, ws); return; }
  b -= N_DN;
  if (p.mask & 64) role_TK(p, b, smem, ws);
}

static void launch_all(void* const* d_in, void* d_out, void* d_ws, const int* tune, const unsigned* masks, int nmasks, int do_pre, hipStream_t stream) {
  Params p;
  p.x = (const uint16_t*)d_in[0]; p.mem0 = (const uint16_t*)d_in[1];
  p.Wq = (const uint16_t*)d_in[2]; p.Wk = (const uint16_t*)d_in[3]; p.Wv = (const uint16_t*)d_in[4]; p.Wo = (const uint16_t*)d_in[5];
  p.Wg = (const uint16_t*)d_in[8]; p.Wu = (const uint16_t*)d_in[9]; p.Wd = (const uint16_t*)d_in[10];
  p.out = (uint16_t*)d_out; p.ws = (char*)d_ws;
  p.nsteps = NSEG; p.dS = tune[0]; p.dVm = tune[1]; p.dVe = tune[2]; p.dVl = tune[3]; p.inc = tune[4]; p.dec = tune[5]; p.mask = 0;
  (void)hipFuncSetAttribute((const void*)k_main, hipFuncAttributeMaxDynamicSharedMemorySize, 160 * 1024);
  if (do_pre) k_pre<<<PB_B + PB_LN + PB_FILL, 256, 0, stream>>>(p);
  for (int i = 0; i < nmasks; i++) { p.mask = masks[i]; k_main<<<256, 256, 160 * 1024, stream>>>(p); }
}
extern "C" void debug_launch(void* const* d_in, void* d_out, void* d_ws, const int* tune, const unsigned* masks, int nmasks, int do_pre, hipStream_t stream) {
  launch_all(d_in, d_out, d_ws, tune, masks, nmasks, do_pre, stream);
}
extern "C" void kernel_launch(void* const* d_in, const int* in_sizes, int n_in, void* d_out, int out_size, void* d_ws, size_t ws_size, hipStream_t stream) {
  const int tune[6] = {2600, 1300, 1500, 2200, 64, 4};
  const unsigned masks[1] = {127};
  launch_all(d_in, d_out, d_ws, tune, masks, 1, 1, stream);
}
```

```cpp
#include <hip/hip_runtime.h>
#include <stdint.h>
#include <type_traits>
typedef __attribute__((ext_vector_type(8), may_alias)) __bf16 bf16x8;
typedef __attribute__((ext_vector_type(16), may_alias)) __bf16 bf16x16;
typedef __attribute__((ext_vector_type(4), may_alias)) __bf16 bf16x4;
typedef __attribute__((ext_vector_type(2), may_alias)) __bf16 bf16x2;
typedef __attribute__((ext_vector_type(4), may_alias)) float f32x4;
typedef __attribute__((ext_vector_type(2), may_alias)) float f32x2;
typedef __attribute__((ext_vector_type(4), may_alias)) unsigned u32x4;
typedef __attribute__((ext_vector_type(2), may_alias)) unsigned u32x2;
typedef __amdgpu_buffer_rsrc_t rsrc_t;
#define LAS __attribute__((address_space(3)))
#define SENT 0xFFFFFFFFu
#define DEVI __device__ __forceinline__

constexpr int NSEG = 64;
constexpr unsigned OFF_FLAGS = 0;
constexpr unsigned OFF_MEMB = 1u << 20;
constexpr unsigned OFF_EBUF = OFF_MEMB + 768 * 1024;
constexpr unsigned OFF_PMB = OFF_EBUF + (1u << 20);
constexpr unsigned OFF_ALB = OFF_PMB + 256 * 1024;
constexpr unsigned OFF_TB = OFF_ALB + 64 * 1024;
constexpr unsigned SENT_END = OFF_TB + 32 * 1024;
constexpr unsigned OFF_XNF = 4u << 20;
constexpr unsigned OFF_QF = 8u << 20;
constexpr unsigned SZ_QF = 64u * 192 * 2 * 1024;
constexpr unsigned OFF_KF = OFF_QF + SZ_QF;
constexpr unsigned OFF_VF = OFF_QF + 2 * SZ_QF;
constexpr unsigned OFF_KM = OFF_QF + 3 * SZ_QF;
constexpr unsigned OFF_BF = OFF_KM + 64 * 32 * 6 * 512;
constexpr unsigned OFF_Z = OFF_BF + 80 * 16 * 10 * 2048;
constexpr unsigned OFF_PX = OFF_Z + 64u * 80 * 32 * 5 * 128;
constexpr unsigned OFF_HF = OFF_PX + 64 * 160 * 512;
constexpr unsigned OFF_G = OFF_HF + 64 * 96 * 1024;
constexpr unsigned OFF_TS = OFF_G + 2048 * 640 * 2;
constexpr unsigned OFF_REC = 32768;
constexpr unsigned OFF_BRDY = 49152;
constexpr unsigned OFF_YF = 65536;
constexpr unsigned OFF_KMF = 131072;
constexpr unsigned FLAGS_END = OFF_KMF + 64 * 32 * 128;
constexpr int N_S = 32, N_V = 80, N_QKV = 58, N_VO = 32, N_GU = 12, N_DN = 10, N_TK = 32;

struct Params {
  const uint16_t* x; const uint16_t* mem0;
  const uint16_t* Wq; const uint16_t* Wk; const uint16_t* Wv; const uint16_t* Wo;
  const uint16_t* Wg; const uint16_t* Wu; const uint16_t* Wd;
  uint16_t* out;
  char* ws;
  int nsteps; int dS, dVm, dVe, dVl, inc, dec; unsigned mask;
};

DEVI int lane_id() { int l; asm volatile("v_mbcnt_lo_u32_b32 %0, -1, 0\n\tv_mbcnt_hi_u32_b32 %0, -1, %0" : "=v"(l)); return l; }
DEVI u32x4 ld_sc1(rsrc_t r, unsigned off) { return __builtin_amdgcn_raw_buffer_load_b128(r, off, 0, 16); }
DEVI unsigned ld1_sc1(rsrc_t r, unsigned off) { return __builtin_amdgcn_raw_buffer_load_b32(r, off, 0, 16); }
DEVI u32x4 ld_plain(rsrc_t r, unsigned off) { return __builtin_amdgcn_raw_buffer_load_b128(r, off, 0, 0); }
DEVI void st_sc1(rsrc_t r, unsigned off, u32x4 v) { __builtin_amdgcn_raw_buffer_store_b128(v, r, off, 0, 16); }
DEVI void st2_sc1(rsrc_t r, unsigned off, u32x2 v) { __builtin_amdgcn_raw_buffer_store_b64(v, r, off, 0, 16); }
DEVI void st1_sc1(rsrc_t r, unsigned off, unsigned v) { __builtin_amdgcn_raw_buffer_store_b32(v, r, off, 0, 16); }
DEVI unsigned mx4(u32x4 v) { return max(max(v.x, v.y), max(v.z, v.w)); }
DEVI unsigned pk2(float a, float b) { bf16x2 v = {(__bf16)a, (__bf16)b}; return __builtin_bit_cast(unsigned, v); }
DEVI unsigned san(unsigned v) { return min(v, 0xFFFFFFFEu); }
DEVI bf16x4 trread(LAS char* p) { return __builtin_amdgcn_ds_read_tr16_b64_v4bf16((LAS bf16x4*)p); }
DEVI float fmx(float a, float b) { float r; asm("v_max_f32 %0, %1, %2" : "=v"(r) : "v"(a), "v"(b)); return r; }
DEVI float fmx3(float a, float b, float c) { float r; asm("v_max3_f32 %0, %1, %2, %3" : "=v"(r) : "v"(a), "v"(b), "v"(c)); return r; }
template <bool AG> DEVI void smf(f32x4& acc, bf16x8 a, const bf16x16& b, int idx) {
  if constexpr (AG) asm volatile("v_smfmac_f32_16x16x64_bf16 %0, %1, %2, %3" : "+v"(acc) : "v"(a), "a"(b), "v"(idx));
  else asm volatile("v_smfmac_f32_16x16x64_bf16 %0, %1, %2, %3" : "+v"(acc) : "v"(a), "v"(b), "v"(idx));
}
DEVI f32x4 zero4() { float a, b, c, d; asm volatile("v_mov_b32 %0, 0" : "=v"(a)); asm volatile("v_mov_b32 %0, 0" : "=v"(b)); asm volatile("v_mov_b32 %0, 0" : "=v"(c)); asm volatile("v_mov_b32 %0, 0" : "=v"(d)); return (f32x4){a, b, c, d}; }
DEVI float ror8add(float x) { return x + __uint_as_float(__builtin_amdgcn_update_dpp(0u, __float_as_uint(x), 0x128, 0xf, 0xf, false)); }
DEVI float xor16max(float x) { auto r = __builtin_amdgcn_permlane16_swap(__float_as_uint(x), __float_as_uint(x), false, false); unsigned a = r[0], b = r[1]; return fmx(__uint_as_float(a), __uint_as_float(b)); }
DEVI float xor32max(float x) { auto r = __builtin_amdgcn_permlane32_swap(__float_as_uint(x), __float_as_uint(x), false, false); unsigned a = r[0], b = r[1]; return fmx(__uint_as_float(a), __uint_as_float(b)); }
DEVI float xor16add(float x) { auto r = __builtin_amdgcn_permlane16_swap(__float_as_uint(x), __float_as_uint(x), false, false); unsigned a = r[0], b = r[1]; return __uint_as_float(a) + __uint_as_float(b); }
DEVI float xor32add(float x) { auto r = __builtin_amdgcn_permlane32_swap(__float_as_uint(x), __float_as_uint(x), false, false); unsigned a = r[0], b = r[1]; return __uint_as_float(a) + __uint_as_float(b); }
DEVI f32x4 halfsum2(f32x4 A, f32x4 B) {
  f32x4 o;
#pragma unroll
  for (int r = 0; r < 4; r++) {
    float a = A[r], b = B[r];
    auto s = __builtin_amdgcn_permlane32_swap(__float_as_uint(a), __float_as_uint(b), false, false);
    unsigned s0 = s[0], s1 = s[1];
    o[r] = __uint_as_float(s0) + __uint_as_float(s1);
  }
  return o;
}
#define STAMP(role, k)
#define STAMP_DECL
#define STAMP_BEGIN
#define STAMP_FLUSH(role)
#define RT_DECL
#define RTS(k)
#define RTFAIL(bit, f)
#define RTF(cu)
#define TL(id, cond)
#define GST(role, seg, k, cond)
constexpr unsigned SPIN_CAP = 1u << 22;

DEVI unsigned now32() { unsigned long long t; asm volatile("s_memtime %0\n\ts_waitcnt lgkmcnt(0)" : "=s"(t) :: "memory"); return (unsigned)t; }
DEVI void wait_until(unsigned t) { while ((int)(now32() - t) < 0) { } }
DEVI unsigned rt32() { unsigned long long t; asm volatile("s_memrealtime %0\n\ts_waitcnt lgkmcnt(0)" : "=s"(t) :: "memory"); return (unsigned)t; }
DEVI void wait_rt(unsigned t) { while ((int)(rt32() - t) < 0) { } }
DEVI int wave_max32(int v) {
  const int lo = (int)0x80000000;
  v = max(v, __builtin_amdgcn_update_dpp(lo, v, 0x111, 0xf, 0xf, false));
  v = max(v, __builtin_amdgcn_update_dpp(lo, v, 0x112, 0xf, 0xf, false));
  v = max(v, __builtin_amdgcn_update_dpp(lo, v, 0x114, 0xf, 0xf, false));
  v = max(v, __builtin_amdgcn_update_dpp(lo, v, 0x118, 0xf, 0xf, false));
  v = max(v, __builtin_amdgcn_update_dpp(lo, v, 0x142, 0xa, 0xf, false));
  return __builtin_amdgcn_readlane(v, 31);
}
DEVI unsigned beacon_max(rsrc_t ws, int s, int lane, unsigned ref) {
  unsigned b = __builtin_amdgcn_raw_buffer_load_b32(ws, (lane & 31) * 16, OFF_TB + s * 512, 16);
  if (__builtin_amdgcn_ballot_w64(b == SENT) != 0) {
    for (unsigned spins = 0; spins < SPIN_CAP; spins++) {
      asm volatile("" ::: "memory");
      b = __builtin_amdgcn_raw_buffer_load_b32(ws, (lane & 31) * 16, OFF_TB + s * 512, 16);
      if (__builtin_amdgcn_ballot_w64(b == SENT) == 0) break;
    }
  }
  return ref + (unsigned)wave_max32((int)(b - ref));
}
DEVI void adapt(int& D, bool fail, int inc, int dec) { D += fail ? inc : -dec; }

DEVI u32x4 ldx(rsrc_t r, unsigned voff, unsigned soff) { return __builtin_amdgcn_raw_buffer_load_b128(r, voff, soff, 16); }
DEVI bool has_sent(unsigned m) { return __builtin_amdgcn_ballot_w64(m == SENT) != 0; }
#define SB() __builtin_amdgcn_sched_barrier(0)
DEVI bool gather_mem(rsrc_t ws, int s, LAS char* mlw, int w, unsigned voff) {
  const unsigned so = OFF_MEMB + s * 10240;
  bool fail = false;
  if (w < 2) {
    u32x4 v0 = ldx(ws, voff, so), v1 = ldx(ws, voff + 4096, so), v2 = ldx(ws, voff + 8192, so);
    SB();
    if (has_sent(max(max(mx4(v0), mx4(v1)), mx4(v2)))) {
      fail = true;
      for (unsigned spins = 0; spins < SPIN_CAP; spins++) {
        asm volatile("" ::: "memory");
        v0 = ldx(ws, voff, so); v1 = ldx(ws, voff + 4096, so); v2 = ldx(ws, voff + 8192, so);
        SB();
        if (!has_sent(max(max(mx4(v0), mx4(v1)), mx4(v2)))) break;
      }
    }
    *(LAS u32x4*)(mlw) = v0; *(LAS u32x4*)(mlw + 4096) = v1; *(LAS u32x4*)(mlw + 8192) = v2;
  } else {
    u32x4 v0 = ldx(ws, voff, so), v1 = ldx(ws, voff + 4096, so);
    SB();
    if (has_sent(max(mx4(v0), mx4(v1)))) {
      fail = true;
      for (unsigned spins = 0; spins < SPIN_CAP; spins++) {
        asm volatile("" ::: "memory");
        v0 = ldx(ws, voff, so); v1 = ldx(ws, voff + 4096, so);
        SB();
        if (!has_sent(max(mx4(v0), mx4(v1)))) break;
      }
    }
    *(LAS u32x4*)(mlw) = v0; *(LAS u32x4*)(mlw + 4096) = v1;
  }
  return fail;
}

DEVI bf16x16 cat16(bf16x8 lo, bf16x8 hi) { return __builtin_shufflevector(lo, hi, 0, 1, 2, 3, 4, 5, 6, 7, 8, 9, 10, 11, 12, 13, 14, 15); }

DEVI bf16x16 tr_frag(LAS char* tile, int ldb, int c0, int lane) {
  const int bg = lane >> 4, q4 = (lane & 15) >> 2, p4 = lane & 3;
  bf16x4 e4[4];
#pragma unroll
  for (int jj = 0; jj < 4; jj++) {
    int g = 2 * (jj >> 1) + (bg >> 1), pp = 2 * (bg & 1) + (jj & 1);
    int row = 8 * g + 2 * pp + (q4 & 1) + 32 * (q4 >> 1);
    e4[jj] = trread(tile + row * ldb + (row >> 5) * 32 + (c0 + 4 * p4) * 2);
  }
  bf16x16 f;
#pragma unroll
  for (int jj = 0; jj < 4; jj++)
#pragma unroll
    for (int e = 0; e < 4; e++) f[4 * jj + e] = e4[jj][e];
  return f;
}

struct AFr { bf16x8 a[10]; };
DEVI void a_load(AFr& A, LAS char* ml, int aoff) {
#pragma unroll
  for (int q = 0; q < 10; q++) A.a[q] = *(LAS bf16x8*)(ml + q * 1024 + aoff);
}
DEVI bf16x16 fb_load(LAS char* wl, int kk, int lane) { return cat16(*(LAS bf16x8*)(wl + kk * 2048 + lane * 16), *(LAS bf16x8*)(wl + kk * 2048 + 1024 + lane * 16)); }
template <int FI> DEVI void smfw(f32x4& acc, bf16x8 a, const bf16x16& b, int idx) { smf<(FI < 32)>(acc, a, b, idx); }
DEVI void acc_zero2(f32x4& a, f32x4& b) { a = (f32x4){0, 0, 0, 0}; b = (f32x4){0, 0, 0, 0}; }
DEVI void lds_signal(LAS char* c, int w, unsigned v) { asm volatile("" ::: "memory"); *(volatile LAS unsigned*)(c + w * 4) = v; asm volatile("" ::: "memory"); }
DEVI void lds_wait3(LAS char* c, unsigned target) {
  while (true) {
    asm volatile("" ::: "memory");
    u32x4 v = *(volatile LAS u32x4*)c;
    unsigned m = min(min(v.y, v.z), v.w);
    if (__builtin_amdgcn_readfirstlane(m) >= target) break;
  }
  asm volatile("" ::: "memory");
}
constexpr unsigned OOB = 0x80000000u;

DEVI void glds16(const void* gsrc, unsigned lds_dst) {
  unsigned keep;
  asm volatile("s_mov_b32 %0, m0\n\ts_mov_b32 m0, %2\n\ts_nop 0\n\tglobal_load_lds_dwordx4 %1, off\n\ts_mov_b32 m0, %0" : "=&s"(keep) : "v"(gsrc), "s"(lds_dst) : "memory");
}
DEVI void drain() { asm volatile("s_waitcnt vmcnt(0)" ::: "memory"); }
DEVI void atom_add(char* ws, unsigned off, unsigned v) { __hip_atomic_fetch_add((unsigned*)(ws + off), v, __ATOMIC_RELAXED, __HIP_MEMORY_SCOPE_AGENT); }
DEVI void wait_flags(rsrc_t ws, unsigned off, unsigned target) {
  unsigned spins = 0;
  while (true) {
    asm volatile("" ::: "memory");
    unsigned v = ld1_sc1(ws, off);
    if (__all(v >= target) || spins > SPIN_CAP) break;
    ++spins;
    __builtin_amdgcn_s_sleep(2);
  }
  asm volatile("" ::: "memory");
}

DEVI void role_S(const Params& p, const int wv, int h, LAS char* smem, rsrc_t ws) {
  const int w = wv;
  if (w == 0) {
    int tid = wv * 64 + lane_id(); asm volatile("; roleSA" : "+v"(tid) :: "memory");

  const int lane = tid & 63;
  const bool stampme = (h == 0);
  LAS char* wl = smem;
  LAS char* stg = smem + 122880;
  LAS char* ml = smem + 122880;
  LAS char* qk = ml + 10240;
  LAS char* kl = qk + 6144;
  LAS char* wlw = wl + (w - 1) * 40960;
  u32x4 pre[6][6];
  rsrc_t rq = __builtin_amdgcn_make_buffer_rsrc((void*)p.Wq, 0, 640 * 6144 * 2, 0x00020000);
  rsrc_t rk = __builtin_amdgcn_make_buffer_rsrc((void*)p.Wk, 0, 640 * 6144 * 2, 0x00020000);
  auto issue = [&](int it) {
    const int mat = it / 10, kk = it % 10;
#pragma unroll
    for (int i = 0; i < 6; i++) {
      int u = i * 256 + tid; int row = u / 24, pc = u - row * 24;
      pre[it % 6][i] = __builtin_amdgcn_raw_buffer_load_b128(mat ? rk : rq, row * 12288 + pc * 16, kk * 64 * 12288 + h * 384, 0);
    }
  };
  const int idx = (lane & 8) ? 0xEEEE : 0x4444;
  const int aoff = ((lane >> 3) & 1) * 512 + (lane >> 4) * 128 + (lane & 7) * 16;
  const float cs = 0.07216878364870322f * 1.4426950408889634f;
  int D = p.dS * 4;
  unsigned tref = 0;
    bf16x16 W[3][10];
    issue(0); issue(1); issue(2); issue(3); issue(4); issue(5);
#pragma unroll
    for (int it = 0; it < 20; it++) {
      const int mat = it / 10, kk = it % 10;
#pragma unroll
      for (int i = 0; i < 6; i++) { const int u = i * 256 + tid, row = u / 24, pc = u - row * 24; *(LAS u32x4*)(stg + row * 400 + (row >> 5) * 32 + pc * 16) = pre[it % 6][i]; }
      __syncthreads();
      if (it + 6 < 20) issue(it + 6);
      if (mat == 0) {
#pragma unroll
        for (int t = 0; t < 3; t++) W[t][kk] = tr_frag(stg, 400, 16 * t, lane);
      }
      __syncthreads();
    }
    const unsigned eoff = ((lane & 15) < 8) ? (unsigned)((lane >> 4) * 128 + (lane & 7) * 16) : OOB;
    const unsigned poff = ((lane & 15) < 8 && lane < 32) ? (unsigned)((lane & 7) * 16 + (lane >> 4) * 8) : OOB;
    const unsigned aloff = (lane < 8) ? (unsigned)(lane * 16) : OOB;
    TL(1, h == 0);
    u32x4 kx[12];
    const unsigned kfoff = lane == 0 ? (unsigned)((32 + h) * 4) : OOB, kftgt = lane == 0 ? 6u : 0u;
    wait_flags(ws, kfoff, kftgt);
#pragma unroll
    for (int i = 0; i < 12; i++) kx[i] = ld_sc1(ws, OFF_KF + (0 * 32 + h) * 12288 + i * 1024 + lane * 16);
    drain(); TL(2, h == 0);
    const int g = lane >> 4, q4 = (lane & 15) >> 2, p4 = lane & 3;
    LAS char* qw = qk + (lane & 15) * 32 + (lane >> 4) * 8;
    for (int s = 0; s < p.nsteps; s++) {
      STAMP_DECL STAMP_BEGIN STAMP(w, 0);
      RT_DECL
      if (s > 0) wait_rt(tref + (D >> 2));
      RTS(0);
      bool fail = gather_mem(ws, s, ml + w * 1024 + lane * 16, w, w * 1024 + lane * 16);
      RTS(1); RTFAIL(1, fail);
      if (s > 0) adapt(D, fail, p.inc, p.dec);
      STAMP(w, 1);
      __syncthreads();
      unsigned long long tb1;
      asm volatile("s_memrealtime %0" : "=s"(tb1));
      RTS(2);
      STAMP(w, 2);
      AFr A; a_load(A, ml, aoff);
      f32x4 a0 = {0, 0, 0, 0}, a1 = {0, 0, 0, 0}, a2 = {0, 0, 0, 0};
      asm volatile("s_nop 1" : "+v"(a0), "+v"(a1), "+v"(a2));
#pragma unroll
      for (int kk = 0; kk < 10; kk++) {
        smf<true>(a0, A.a[kk], W[0][kk], idx); smf<true>(a1, A.a[kk], W[1][kk], idx); smf<true>(a2, A.a[kk], W[2][kk], idx);
        if (kk == 4) { asm volatile("s_waitcnt lgkmcnt(0)" : "+s"(tb1)); __builtin_amdgcn_raw_buffer_store_b32(san((unsigned)tb1), ws, lane == 0 ? 0u : OOB, OFF_TB + s * 512 + h * 16, 16); }
      }
      asm volatile("s_nop 7" : "+v"(a0), "+v"(a1), "+v"(a2));
      STAMP(w, 3);
      *(LAS u32x2*)(qw) = (u32x2){pk2(a0[0], a0[1]), pk2(a0[2], a0[3])};
      *(LAS u32x2*)(qw + 512) = (u32x2){pk2(a1[0], a1[1]), pk2(a1[2], a1[3])};
      *(LAS u32x2*)(qw + 1024) = (u32x2){pk2(a2[0], a2[1]), pk2(a2[2], a2[3])};
      __syncthreads();
      STAMP(w, 4);
      bf16x8 qf[6];
#pragma unroll
      for (int ks = 0; ks < 6; ks++) {
        int e0 = 32 * ks + 8 * g + q4;
        bf16x4 qa = trread(qk + e0 * 32 + p4 * 8), qb = trread(qk + (e0 + 4) * 32 + p4 * 8);
        qf[ks] = __builtin_shufflevector(qa, qb, 0, 1, 2, 3, 4, 5, 6, 7);
      }
      f32x4 T1, T2;
      asm volatile("s_nop 1\n\tv_mfma_f32_16x16x32_bf16 %0, %2, %8, 0\n\tv_mfma_f32_16x16x32_bf16 %1, %3, %8, 0\n\t"
                   "v_mfma_f32_16x16x32_bf16 %0, %4, %9, %0\n\tv_mfma_f32_16x16x32_bf16 %1, %5, %9, %1\n\t"
                   "v_mfma_f32_16x16x32_bf16 %0, %6, %10, %0\n\tv_mfma_f32_16x16x32_bf16 %1, %7, %10, %1"
                   : "=&v"(T1), "=&v"(T2) : "v"(kx[0]), "v"(kx[1]), "v"(kx[2]), "v"(kx[3]), "v"(kx[4]), "v"(kx[5]), "v"(qf[0]), "v"(qf[1]), "v"(qf[2]));
      asm volatile("s_nop 1\n\tv_mfma_f32_16x16x32_bf16 %0, %2, %8, %0\n\tv_mfma_f32_16x16x32_bf16 %1, %3, %8, %1\n\t"
                   "v_mfma_f32_16x16x32_bf16 %0, %4, %9, %0\n\tv_mfma_f32_16x16x32_bf16 %1, %5, %9, %1\n\t"
                   "v_mfma_f32_16x16x32_bf16 %0, %6, %10, %0\n\tv_mfma_f32_16x16x32_bf16 %1, %7, %10, %1\n\ts_nop 7"
                   : "+v"(T1), "+v"(T2) : "v"(kx[6]), "v"(kx[7]), "v"(kx[8]), "v"(kx[9]), "v"(kx[10]), "v"(kx[11]), "v"(qf[3]), "v"(qf[4]), "v"(qf[5]));
      STAMP(w, 5);
#pragma unroll
      for (int r = 0; r < 4; r++) { T1[r] = ror8add(T1[r]); T2[r] = ror8add(T2[r]); }
      float mx = fmx3(fmx3(T1[0], T1[1], T1[2]), fmx3(T1[3], T2[0], T2[1]), fmx(T2[2], T2[3]));
      mx = xor32max(xor16max(mx));
      const float mxc = mx * cs;
      float E1[4], E2[4], lx = 0;
#pragma unroll
      for (int r = 0; r < 4; r++) { E1[r] = __builtin_fmaf(T1[r], cs, -mxc); E2[r] = __builtin_fmaf(T2[r], cs, -mxc); }
      SB();
#pragma unroll
      for (int r = 0; r < 4; r++) { E1[r] = __builtin_amdgcn_exp2f(E1[r]); E2[r] = __builtin_amdgcn_exp2f(E2[r]); }
      SB();
      {
        u32x4 ev = {san(pk2(E1[0], E1[1])), san(pk2(E1[2], E1[3])), san(pk2(E2[0], E2[1])), san(pk2(E2[2], E2[3]))};
        __builtin_amdgcn_raw_buffer_store_b128(ev, ws, eoff, OFF_EBUF + (s * 32 + h) * 512, 16);
      }
      RTS(3);
      STAMP(w, 6);
      lx = ((E1[0] + E1[1]) + (E1[2] + E1[3])) + ((E2[0] + E2[1]) + (E2[2] + E2[3]));
      lx = xor32add(xor16add(lx));
      __syncthreads();
      STAMP(w, 7);
      bf16x8 kmf[6];
#pragma unroll
      for (int ks = 0; ks < 6; ks++) {
        int e0 = 32 * ks + 8 * g + q4;
        bf16x4 ka = trread(kl + e0 * 16 + (p4 & 1) * 8), kb = trread(kl + (e0 + 4) * 16 + (p4 & 1) * 8);
        kmf[ks] = __builtin_shufflevector(ka, kb, 0, 1, 2, 3, 4, 5, 6, 7);
      }
      f32x4 T0;
      asm volatile("s_nop 1\n\tv_mfma_f32_16x16x32_bf16 %0, %1, %4, 0\n\tv_mfma_f32_16x16x32_bf16 %0, %2, %5, %0\n\tv_mfma_f32_16x16x32_bf16 %0, %3, %6, %0"
                   : "=&v"(T0) : "v"(kmf[0]), "v"(kmf[1]), "v"(kmf[2]), "v"(qf[0]), "v"(qf[1]), "v"(qf[2]));
      asm volatile("s_nop 1\n\tv_mfma_f32_16x16x32_bf16 %0, %1, %4, %0\n\tv_mfma_f32_16x16x32_bf16 %0, %2, %5, %0\n\tv_mfma_f32_16x16x32_bf16 %0, %3, %6, %0\n\ts_nop 7"
                   : "+v"(T0) : "v"(kmf[3]), "v"(kmf[4]), "v"(kmf[5]), "v"(qf[3]), "v"(qf[4]), "v"(qf[5]));
#pragma unroll
      for (int r = 0; r < 4; r++) T0[r] = ror8add(T0[r]);
      float mm = xor16max(fmx(fmx3(T0[0], T0[1], T0[2]), T0[3]));
      float m = fmx(mx, mm);
      const float mc = m * cs;
      float ax = __builtin_amdgcn_exp2f(mxc - mc);
      float E0[4], lm;
#pragma unroll
      for (int r = 0; r < 4; r++) E0[r] = __builtin_amdgcn_exp2f(__builtin_fmaf(T0[r], cs, -mc));
      lm = xor16add((E0[0] + E0[1]) + (E0[2] + E0[3]));
      float inv = __builtin_amdgcn_rcpf(lx * ax + lm);
      {
        u32x2 pv = {san(pk2(E0[0] * inv, E0[1] * inv)), san(pk2(E0[2] * inv, E0[3] * inv))};
        __builtin_amdgcn_raw_buffer_store_b64(pv, ws, poff, OFF_PMB + (s * 32 + h) * 128, 16);
        __builtin_amdgcn_raw_buffer_store_b32(san(__float_as_uint(ax * inv)), ws, aloff, OFF_ALB + s * 1024 + ((h >> 3) * 2 + (h & 1)) * 128 + ((h >> 1) & 3) * 4, 16);
      }
      RTS(4);
      STAMP(w, 8);
#pragma unroll
      for (int ks = 0; ks < 6; ks++) __builtin_amdgcn_raw_buffer_store_b128(__builtin_bit_cast(u32x4, kmf[ks]), ws, eoff, OFF_KM + ((s * 32 + h) * 6 + ks) * 512, 16);
      if (s + 1 < NSEG) {
        wait_flags(ws, kfoff == OOB ? OOB : kfoff + (s + 1) * 512, kftgt);
        __builtin_amdgcn_raw_buffer_store_b32(1u, ws, lane == 0 ? 0u : OOB, OFF_KMF + (s * 32 + h) * 128, 16);
#pragma unroll
        for (int i = 0; i < 12; i++) kx[i] = ld_sc1(ws, OFF_KF + ((s + 1) * 32 + h) * 12288 + i * 1024 + lane * 16);
      }
      tref = beacon_max(ws, s, lane, (unsigned)tb1);
      RTF(h);
      STAMP_FLUSH(w);
      if (lane == 0 && s == p.nsteps - 1) { unsigned* d = (unsigned*)(p.ws + OFF_TS + 40960 + h * 16); d[0] = D; }
      if (tid == 0 && h == 0) { long long t = wall_clock64(); __builtin_amdgcn_raw_buffer_store_b64(__builtin_bit_cast(u32x2, t), ws, OFF_TS + s * 8, 0, 0); }
    }
    drain(); __builtin_amdgcn_raw_buffer_store_b32(1u, ws, lane == 0 ? 0u : OOB, OFF_KMF + ((p.nsteps - 1) * 32 + h) * 128, 16);
    __builtin_amdgcn_endpgm(); __builtin_unreachable();
  } else {
    int tid = wv * 64 + lane_id(); asm volatile("; roleSB" : "+v"(tid) :: "memory");

  const int lane = tid & 63;
  const bool stampme = (h == 0);
  LAS char* wl = smem;
  LAS char* stg = smem + 122880;
  LAS char* ml = smem + 122880;
  LAS char* qk = ml + 10240;
  LAS char* kl = qk + 6144;
  LAS char* wlw = wl + (w - 1) * 40960;
  u32x4 pre[6][6];
  rsrc_t rq = __builtin_amdgcn_make_buffer_rsrc((void*)p.Wq, 0, 640 * 6144 * 2, 0x00020000);
  rsrc_t rk = __builtin_amdgcn_make_buffer_rsrc((void*)p.Wk, 0, 640 * 6144 * 2, 0x00020000);
  auto issue = [&](int it) {
    const int mat = it / 10, kk = it % 10;
#pragma unroll
    for (int i = 0; i < 6; i++) {
      int u = i * 256 + tid; int row = u / 24, pc = u - row * 24;
      pre[it % 6][i] = __builtin_amdgcn_raw_buffer_load_b128(mat ? rk : rq, row * 12288 + pc * 16, kk * 64 * 12288 + h * 384, 0);
    }
  };
  const int idx = (lane & 8) ? 0xEEEE : 0x4444;
  const int aoff = ((lane >> 3) & 1) * 512 + (lane >> 4) * 128 + (lane & 7) * 16;
  const float cs = 0.07216878364870322f * 1.4426950408889634f;
  int D = p.dS * 4;
  unsigned tref = 0;
    LAS char* qw = qk + (48 * w + (lane & 15)) * 32 + (lane >> 4) * 8;
    bf16x16 W[5][10];
    issue(0); issue(1); issue(2); issue(3); issue(4); issue(5);
#pragma unroll
    for (int it = 0; it < 20; it++) {
      const int mat = it / 10, kk = it % 10;
#pragma unroll
      for (int i = 0; i < 6; i++) { const int u = i * 256 + tid, row = u / 24, pc = u - row * 24; *(LAS u32x4*)(stg + row * 400 + (row >> 5) * 32 + pc * 16) = pre[it % 6][i]; }
      __syncthreads();
      if (it + 6 < 20) issue(it + 6);
      const int nt = mat == 0 ? 3 : 4;
#pragma unroll
      for (int t = 0; t < 4; t++) if (t < nt) {
        bf16x16 f = tr_frag(stg, 400, mat == 0 ? 16 * (3 * w + t) : 16 * (4 * (w - 1) + t), lane);
        if (t < nt - 1) W[mat == 0 ? t : 2 + t][kk] = f;
        else { LAS char* d = wlw + (mat * 10 + kk) * 2048 + lane * 16;
          *(LAS bf16x8*)d = __builtin_shufflevector(f, f, 0, 1, 2, 3, 4, 5, 6, 7);
          *(LAS bf16x8*)(d + 1024) = __builtin_shufflevector(f, f, 8, 9, 10, 11, 12, 13, 14, 15); }
      }
      __syncthreads();
    }
    LAS char* k01addr = kl + (64 * (w - 1) + 16 * (lane >> 5) + (lane & 15)) * 16 + ((lane >> 4) & 1) * 8;
    for (int s = 0; s < p.nsteps; s++) {
      STAMP_DECL STAMP_BEGIN STAMP(w, 0);
      RT_DECL
      if (s > 0) wait_rt(tref + (D >> 2));
      RTS(0);
      bf16x16 fb[4];
#pragma unroll
      for (int q = 0; q < 3; q++) fb[q] = fb_load(wlw, q, lane);
      bool fail = gather_mem(ws, s, ml + w * 1024 + lane * 16, w, w * 1024 + lane * 16);
      RTS(1); RTFAIL(1, fail);
      if (s > 0) adapt(D, fail, p.inc, p.dec);
      STAMP(w, 1);
      __syncthreads();
      RTS(2);
      STAMP(w, 2);
      AFr A; a_load(A, ml, aoff);
      f32x4 a0 = {0, 0, 0, 0}, a1 = {0, 0, 0, 0}, a2 = {0, 0, 0, 0};
      asm volatile("s_nop 1" : "+v"(a0), "+v"(a1), "+v"(a2));
#pragma unroll
      for (int kk = 0; kk < 10; kk++) {
        smfw<0>(a0, A.a[kk], W[0][kk], idx);
        SB();
        bf16x8 lo = *(LAS bf16x8*)(wlw + (kk + 3) * 2048 + lane * 16);
        SB();
        if (10 + kk < 32) smf<true>(a1, A.a[kk], W[1][kk], idx); else smf<false>(a1, A.a[kk], W[1][kk], idx);
        SB();
        bf16x8 hi = *(LAS bf16x8*)(wlw + (kk + 3) * 2048 + 1024 + lane * 16);
        SB();
        smf<false>(a2, A.a[kk], fb[kk % 4], idx);
        fb[(kk + 3) % 4] = cat16(lo, hi);
        SB();
      }
      asm volatile("s_nop 7" : "+v"(a0), "+v"(a1), "+v"(a2));
      STAMP(w, 3);
      *(LAS u32x2*)(qw) = (u32x2){pk2(a0[0], a0[1]), pk2(a0[2], a0[3])};
      *(LAS u32x2*)(qw + 512) = (u32x2){pk2(a1[0], a1[1]), pk2(a1[2], a1[3])};
      *(LAS u32x2*)(qw + 1024) = (u32x2){pk2(a2[0], a2[1]), pk2(a2[2], a2[3])};
      SB();
      STAMP(w, 4);
      f32x4 y0 = {0, 0, 0, 0}, y1 = {0, 0, 0, 0}, x0 = {0, 0, 0, 0}, x1 = {0, 0, 0, 0};
      asm volatile("s_nop 1" : "+v"(y0), "+v"(y1), "+v"(x0), "+v"(x1));
#pragma unroll
      for (int kk = 0; kk < 10; kk++) {
        smf<false>(y0, A.a[kk], W[4][kk], idx);
        SB();
        bf16x8 lo, hi;
        if (kk + 3 < 10) lo = *(LAS bf16x8*)(wlw + (kk + 13) * 2048 + lane * 16);
        SB();
        smf<true>(x0, A.a[kk], W[2][kk], idx);
        SB();
        if (kk + 3 < 10) hi = *(LAS bf16x8*)(wlw + (kk + 13) * 2048 + 1024 + lane * 16);
        SB();
        smf<false>(y1, A.a[kk], fb[(kk + 10) % 4], idx);
        if (kk + 3 < 10) fb[(kk + 13) % 4] = cat16(lo, hi);
        SB();
        if (kk == 0) { asm volatile("s_waitcnt lgkmcnt(2)\n\ts_barrier" ::: "memory"); SB(); }
      }
      unsigned sw[4][2]; f32x4 oy;
#pragma unroll
      for (int kk = 0; kk < 10; kk++) {
        if (kk < 2) smf<true>(x1, A.a[kk], W[3][kk], idx); else smf<false>(x1, A.a[kk], W[3][kk], idx);
        SB();
        if (kk == 4) asm volatile("" : "+v"(y0), "+v"(y1));
        if (kk >= 4 && kk < 8) { const int r = kk - 4; float ya = y0[r], yb = y1[r]; auto t = __builtin_amdgcn_permlane32_swap(__float_as_uint(ya), __float_as_uint(yb), false, false); sw[r][0] = t[0]; sw[r][1] = t[1];
          if (r > 0) oy[r - 1] = __uint_as_float(sw[r - 1][0]) + __uint_as_float(sw[r - 1][1]); }
        if (kk == 8) { oy[3] = __uint_as_float(sw[3][0]) + __uint_as_float(sw[3][1]); }
        if (kk == 9) *(LAS u32x2*)(k01addr + 512) = (u32x2){pk2(oy[0], oy[1]), pk2(oy[2], oy[3])};
        SB();
      }
      asm volatile("s_nop 7" : "+v"(x0), "+v"(x1));
      STAMP(w, 5);
      {
        f32x4 o = halfsum2(x0, x1);
        *(LAS u32x2*)(k01addr) = (u32x2){pk2(o[0], o[1]), pk2(o[2], o[3])};
      }
      __syncthreads();
      tref = beacon_max(ws, s, lane, rt32());
      RTF(h);
      STAMP(w, 7);
      STAMP_FLUSH(w);
    }
    __builtin_amdgcn_endpgm(); __builtin_unreachable();
  }
}

DEVI void role_V(const Params& p, const int wv, int j, LAS char* smem, rsrc_t ws) {
  int tid = wv * 64 + lane_id(); asm volatile("; roleV" : "+v"(tid) :: "memory");
  const int lane = tid & 63, w = __builtin_amdgcn_readfirstlane(tid >> 6);
  const bool stampme = (j == 0);
  LAS char* ml = smem;
  LAS char* yl = smem + 10240;
  LAS char* red = smem + 10240 + 4096;
  bf16x16 B[4][10];
  wait_flags(ws, lane == 0 ? (unsigned)(OFF_BRDY + (j / 20) * 128) : OOB, lane == 0 ? 32u : 0u);
#pragma unroll
  for (int t = 0; t < 4; t++)
#pragma unroll
    for (int kk = 0; kk < 10; kk++) {
      unsigned o = OFF_BF + ((j * 16 + 4 * w + t) * 10 + kk) * 2048 + lane * 16;
      B[t][kk] = cat16(__builtin_bit_cast(bf16x8, ld_sc1(ws, o)), __builtin_bit_cast(bf16x8, ld_sc1(ws, o + 1024)));
    }
  const int idx = (lane & 8) ? 0xEEEE : 0x4444;
  const int half = (lane >> 3) & 1, g = lane >> 4, i8 = lane & 7;
  const int aoff = half * 512 + g * 128 + i8 * 16;
  const int la = i8 + 16 * ((lane >> 3) & 1);
  const unsigned moff = ((lane & 15) < 8) ? (unsigned)(i8 * 16 + ((lane >> 4) & 1) * 8 + (lane >> 5) * 4) : OOB;
  const unsigned evo = (8 * w + half) * 512 + g * 128 + i8 * 16;
  const unsigned avo = (w * 2 + (g >> 1)) * 128 + i8 * 16;
  const unsigned pvo = (8 * w + 4 * half + g) * 128 + i8 * 16;
  const unsigned yvo = ((j * 32 + 8 * w + (lane >> 3)) * 5) * 128 + i8 * 16;
  const unsigned zvo = ((j * 32 + 8 * w + half) * 5 + 1 + g) * 128 + i8 * 16;
  const unsigned mso = j * 128;
  u32x4 zx[4];
  auto zload = [&](int s) {
#pragma unroll
    for (int q = 0; q < 4; q++) zx[q] = ldx(ws, zvo + q * 1280, OFF_Z + s * (80 * 20480));
  };
  drain(); TL(3, j == 0);
  const unsigned vfoff = lane == 0 ? (unsigned)(OFF_REC + 0) : OOB, vftgt = lane == 0 ? 32u : 0u;
  int Dm = p.dVm * 4, De = p.dVe * 4, Dl = p.dVl * 4;
  unsigned tb = 0;
  unsigned vfl = 0;
  wait_flags(ws, vfoff, vftgt);
  zload(0);
  for (int s = 0; s < p.nsteps; s++) {
    STAMP_DECL STAMP_BEGIN STAMP(4 + w, 0);
    RT_DECL
    if (s > 0) wait_rt(tb + (Dm >> 2));
    RTS(0);
    bool failm = gather_mem(ws, s, ml + w * 1024 + lane * 16, w, w * 1024 + lane * 16);
    RTS(1); RTFAIL(1, failm);
    if (s > 0) adapt(Dm, failm, p.inc, p.dec);
    STAMP(4 + w, 1);
    __syncthreads();
    STAMP(4 + w, 2);
    f32x4 acc[4];
    {
      AFr A; a_load(A, ml, aoff);
#pragma unroll
      for (int t = 0; t < 4; t++) acc[t] = (f32x4){0, 0, 0, 0};
      asm volatile("s_nop 1" : "+v"(acc[0]), "+v"(acc[1]), "+v"(acc[2]), "+v"(acc[3]));
#pragma unroll
      for (int kk = 0; kk < 10; kk++)
#pragma unroll
        for (int t = 0; t < 4; t++) { if (t * 10 + kk < 32) smf<true>(acc[t], A.a[kk], B[t][kk], idx); else smf<false>(acc[t], A.a[kk], B[t][kk], idx); }
      asm volatile("s_nop 7" : "+v"(acc[0]), "+v"(acc[1]), "+v"(acc[2]), "+v"(acc[3]));
    }
#pragma unroll
    for (int pr = 0; pr < 2; pr++) {
      f32x4 o = halfsum2(acc[2 * pr], acc[2 * pr + 1]);
      int t = 2 * pr + (lane >> 5);
      int hl = 2 * t + ((lane >> 3) & 1);
      u32x2 v = {pk2(o[0], o[1]), pk2(o[2], o[3])};
      *(LAS u32x2*)(yl + w * 1024 + (hl * 8 + i8) * 16 + ((lane >> 4) & 1) * 8) = v;
    }
    bf16x8 yf = *(LAS bf16x8*)(yl + w * 1024 + ((4 * half + g) * 8 + i8) * 16);
    u32x4 yv = *(LAS u32x4*)(yl + w * 1024 + lane * 16);
    STAMP(4 + w, 3);
    f32x4 ox[4]; bool znext = false, fe_ = false, fl_ = false;
    {
      u32x4 ev[4]; u32x4 al; u32x4 pm;
      const unsigned se = OFF_EBUF + s * 16384, sa = OFF_ALB + s * 1024, sp = OFF_PMB + s * 4096;
      auto ld_e = [&]() {
#pragma unroll
        for (int q = 0; q < 4; q++) ev[q] = ldx(ws, evo + q * 1024, se);
      };
      auto ld_l = [&]() { al = ldx(ws, avo, sa); pm = ldx(ws, pvo, sp); };
      tb = beacon_max(ws, s, lane, rt32());
      __builtin_amdgcn_raw_buffer_store_b128(yv, ws, yvo, OFF_Z + s * (80 * 20480), 16);
      const unsigned te = tb + (De >> 2), tl = tb + (Dl >> 2);
      SB();
      wait_rt(te);
      RTS(2);
      ld_e();
      vfl = ld1_sc1(ws, (vfoff == OOB || s + 1 >= NSEG) ? OOB : vfoff + (s + 1) * 128);
      SB();
      wait_rt(tl);
      RTS(3);
      ld_l();
      SB();
      bool fe = false, fl = false;
      if (has_sent(max(max(mx4(ev[0]), mx4(ev[1])), max(mx4(ev[2]), mx4(ev[3]))))) {
        fe = true;
        for (unsigned spins = 0; spins < SPIN_CAP; spins++) {
          asm volatile("" ::: "memory");
          ld_e();
          SB();
          if (!has_sent(max(max(mx4(ev[0]), mx4(ev[1])), max(mx4(ev[2]), mx4(ev[3]))))) break;
        }
      }
      asm volatile("s_nop 1\n\tv_mfma_f32_16x16x32_bf16 %0, %4, %8, 0\n\tv_mfma_f32_16x16x32_bf16 %1, %5, %9, 0\n\tv_mfma_f32_16x16x32_bf16 %2, %6, %10, 0\n\tv_mfma_f32_16x16x32_bf16 %3, %7, %11, 0"
                   : "=&v"(ox[0]), "=&v"(ox[1]), "=&v"(ox[2]), "=&v"(ox[3]) : "v"(zx[0]), "v"(zx[1]), "v"(zx[2]), "v"(zx[3]), "v"(ev[0]), "v"(ev[1]), "v"(ev[2]), "v"(ev[3]));
      STAMP(4 + w, 4);
      SB();
      float s01, s23;
      auto late = [&]() {
        f32x4 tm4;
        asm volatile("s_nop 1\n\tv_mfma_f32_16x16x32_bf16 %0, %1, %2, 0" : "=&v"(tm4) : "v"(yf), "v"(pm));
        asm volatile("s_nop 3" : "+v"(ox[0]), "+v"(ox[1]), "+v"(ox[2]), "+v"(ox[3]));
        const float a0 = __uint_as_float(al.x), a1 = __uint_as_float(al.y), a2 = __uint_as_float(al.z), a3 = __uint_as_float(al.w);
        f32x4 u0 = ox[0] * a0, u1 = ox[2] * a2;
        u0 = __builtin_elementwise_fma(ox[1], (f32x4){a1, a1, a1, a1}, u0); u1 = __builtin_elementwise_fma(ox[3], (f32x4){a3, a3, a3, a3}, u1);
        u0 += u1;
        asm volatile("" : "+v"(tm4), "+v"(u0));
        u0 += tm4;
        float t[4] = {u0[0], u0[1], u0[2], u0[3]};
        auto a = __builtin_amdgcn_permlane32_swap(__float_as_uint(t[0]), __float_as_uint(t[1]), false, false);
        auto b = __builtin_amdgcn_permlane32_swap(__float_as_uint(t[2]), __float_as_uint(t[3]), false, false);
        unsigned a0_ = a[0], a1_ = a[1], b0_ = b[0], b1_ = b[1];
        s01 = __uint_as_float(a0_) + __uint_as_float(__builtin_amdgcn_update_dpp(0u, a1_, 0x128, 0xf, 0xf, false));
        s23 = __uint_as_float(b0_) + __uint_as_float(__builtin_amdgcn_update_dpp(0u, b1_, 0x128, 0xf, 0xf, false));
      };
      late();
      if (has_sent(max(mx4(al), mx4(pm)))) {
        fl = true;
        for (unsigned spins = 0; spins < SPIN_CAP; spins++) {
          asm volatile("" ::: "memory");
          ld_l();
          SB();
          if (!has_sent(max(mx4(al), mx4(pm)))) break;
        }
        late();
      }
      RTS(4); RTFAIL(2, fe); RTFAIL(4, fl);
      znext = s + 1 < NSEG; fe_ = fe; fl_ = fl;
      STAMP(4 + w, 5);
      if (w != 0) *(LAS f32x2*)(red + w * 512 + lane * 8) = (f32x2){s01, s23};
      __syncthreads();
      if (w == 1) __builtin_amdgcn_raw_buffer_store_b32(1u, ws, lane == 0 ? (unsigned)(j * 4) : OOB, OFF_YF + s * 512, 16);
      STAMP(4 + w, 6);
      if (w == 0) {
        f32x2 r1 = *(LAS f32x2*)(red + 512 + lane * 8), r2 = *(LAS f32x2*)(red + 1024 + lane * 8), r3 = *(LAS f32x2*)(red + 1536 + lane * 8);
        const float u01 = (s01 + r1[0]) + (r2[0] + r3[0]), u23 = (s23 + r1[1]) + (r2[1] + r3[1]);
        auto c = __builtin_amdgcn_permlane32_swap(__float_as_uint(u01), __float_as_uint(u23), false, false);
        unsigned c0 = c[0], c1 = c[1];
        __builtin_amdgcn_raw_buffer_store_b32(san(pk2(__uint_as_float(c0), __uint_as_float(c1))), ws, moff, OFF_MEMB + (s + 1) * 10240 + j * 128, 16);
      }
    }
    RTS(5);
    adapt(De, fe_, p.inc, p.dec); adapt(Dl, fl_, p.inc, p.dec);
    if (znext) { if (!__all(vfl >= vftgt)) wait_flags(ws, vfoff == OOB ? OOB : vfoff + (s + 1) * 128, vftgt); zload(s + 1); }
    RTF(32 + j);
    STAMP(4 + w, 7);
    STAMP_FLUSH(4 + w);
    if (lane == 0 && s == p.nsteps - 1) { unsigned* d = (unsigned*)(p.ws + OFF_TS + 32768 + (j * 4 + w) * 16); d[0] = Dm; d[1] = De; d[2] = Dl; }
  }
  __builtin_amdgcn_endpgm(); __builtin_unreachable();
}

template <bool AG> DEVI void mf0(f32x4& acc, const bf16x8& a, const bf16x8& b) {
  if constexpr (AG) asm volatile("v_mfma_f32_16x16x32_bf16 %0, %1, %2, 0" : "=&v"(acc) : "a"(a), "v"(b));
  else asm volatile("v_mfma_f32_16x16x32_bf16 %0, %1, %2, 0" : "=&v"(acc) : "v"(a), "v"(b));
}
template <bool AG> DEVI void mfa(f32x4& acc, const bf16x8& a, const bf16x8& b) {
  if constexpr (AG) asm volatile("v_mfma_f32_16x16x32_bf16 %0, %1, %2, %0" : "+v"(acc) : "a"(a), "v"(b));
  else asm volatile("v_mfma_f32_16x16x32_bf16 %0, %1, %2, %0" : "+v"(acc) : "v"(a), "v"(b));
}
template <bool AG> DEVI void mf0B(f32x4& acc, const bf16x8& a, const bf16x8& b) {
  if constexpr (AG) asm volatile("v_mfma_f32_16x16x32_bf16 %0, %1, %2, 0" : "=&v"(acc) : "v"(a), "a"(b));
  else asm volatile("v_mfma_f32_16x16x32_bf16 %0, %1, %2, 0" : "=&v"(acc) : "v"(a), "v"(b));
}
template <bool AG> DEVI void mfaB(f32x4& acc, const bf16x8& a, const bf16x8& b) {
  if constexpr (AG) asm volatile("v_mfma_f32_16x16x32_bf16 %0, %1, %2, %0" : "+v"(acc) : "v"(a), "a"(b));
  else asm volatile("v_mfma_f32_16x16x32_bf16 %0, %1, %2, %0" : "+v"(acc) : "v"(a), "v"(b));
}
DEVI void wait_flags_slow(rsrc_t ws, unsigned off, unsigned target) {
  unsigned spins = 0;
  while (true) {
    asm volatile("" ::: "memory");
    unsigned v = ld1_sc1(ws, off);
    if (__all(v >= target) || spins > SPIN_CAP) break;
    ++spins;
    __builtin_amdgcn_s_sleep(12);
  }
  asm volatile("" ::: "memory");
}
DEVI bf16x8 tr8(LAS char* tile, int ldb, int row0, int colbyte, int lane) {
  const int g = lane >> 4, q4 = (lane & 15) >> 2;
  bf16x4 a = trread(tile + (row0 + 8 * g + q4) * ldb + colbyte), b = trread(tile + (row0 + 8 * g + 4 + q4) * ldb + colbyte);
  return __builtin_shufflevector(a, b, 0, 1, 2, 3, 4, 5, 6, 7);
}
DEVI u32x4 pack8(const f32x4& a, const f32x4& b) { return (u32x4){pk2(a[0], a[1]), pk2(a[2], a[3]), pk2(b[0], b[1]), pk2(b[2], b[3])}; }
DEVI float bf2f(unsigned short v) { return __uint_as_float((unsigned)v << 16); }
DEVI float rbf(float v) { return (float)(__bf16)v; }

DEVI void x_dma(const Params& p, int s, unsigned ldsbase, int w, int lane) {
  const char* src = p.ws + OFF_XNF + s * 40960 + w * 10240 + lane * 16;
#pragma unroll
  for (int i = 0; i < 10; i++) glds16(src + i * 1024, ldsbase + w * 10240 + i * 1024);
}

template <int NT, int NKS, class F>
DEVI void dense_phase(f32x4 (&acc)[NT][2], const bf16x8 (&W)[NT][NKS], LAS char* xb, int lane, F filler) {
  bf16x8 b[3][2];
#pragma unroll
  for (int q = 0; q < 2; q++) { b[q][0] = *(LAS bf16x8*)(xb + (q * 2) * 1024 + lane * 16); b[q][1] = *(LAS bf16x8*)(xb + (q * 2 + 1) * 1024 + lane * 16); }
#pragma unroll
  for (int kk = 0; kk < NKS; kk++) {
#pragma unroll
    for (int t = 0; t < NT; t++)
#pragma unroll
      for (int tt = 0; tt < 2; tt++) {
        if (kk == 0) { if (t * NKS + kk < 64) mf0<true>(acc[t][tt], W[t][kk], b[kk % 3][tt]); else mf0<false>(acc[t][tt], W[t][kk], b[kk % 3][tt]); }
        else { if (t * NKS + kk < 64) mfa<true>(acc[t][tt], W[t][kk], b[kk % 3][tt]); else mfa<false>(acc[t][tt], W[t][kk], b[kk % 3][tt]); }
        SB();
        if (t == 0 && tt == 1 && kk + 2 < NKS) b[(kk + 2) % 3][0] = *(LAS bf16x8*)(xb + ((kk + 2) * 2) * 1024 + lane * 16);
        if (t == 1 && tt == 1 && kk + 2 < NKS) b[(kk + 2) % 3][1] = *(LAS bf16x8*)(xb + ((kk + 2) * 2 + 1) * 1024 + lane * 16);
        if (t == 2 && tt == 1) filler(kk);
        SB();
      }
  }
  asm volatile("s_nop 7" ::: "memory");
#pragma unroll
  for (int t = 0; t < NT; t++) asm volatile("" : "+v"(acc[t][0]), "+v"(acc[t][1]));
}

DEVI void role_QKV(const Params& p, const int wv, int c, LAS char* smem, rsrc_t ws) {
  int tid = wv * 64 + lane_id(); asm volatile("; roleQKV" : "+v"(tid) :: "memory");
  const int lane = tid & 63, w = __builtin_amdgcn_readfirstlane(tid >> 6);
  const unsigned lds0 = (unsigned)(size_t)smem;
  bf16x8 W[5][20];
  x_dma(p, 0, lds0 + 81920, w, lane);
  {
    const int pc = tid % 40, r0 = min(tid / 40, 5);
    const int gcol = min(320 * c + 8 * pc, 18432 - 8);
    const int mat = gcol / 6144, cm = gcol - mat * 6144;
    const uint16_t* base = (mat == 0 ? p.Wq : mat == 1 ? p.Wk : p.Wv) + cm;
    u32x4 pre[3][11];
    auto issue = [&](int ch) {
#pragma unroll
      for (int i = 0; i < 11; i++) { int r = min(r0 + 6 * i, 63); pre[ch % 3][i] = *(const u32x4*)(base + (size_t)(64 * ch + r) * 6144); }
    };
    issue(0); issue(1); issue(2);
    const int p4 = lane & 3;
#pragma unroll
    for (int ch = 0; ch < 10; ch++) {
#pragma unroll
      for (int i = 0; i < 11; i++) { int r = min(r0 + 6 * i, 63); *(LAS u32x4*)(smem + r * 656 + pc * 16) = pre[ch % 3][i]; }
      __syncthreads();
      if (ch + 3 < 10) issue(ch + 3);
#pragma unroll
      for (int k2 = 0; k2 < 2; k2++)
#pragma unroll
        for (int t = 0; t < 5; t++) {
          const int blk = t < 4 ? 2 * w + (t >> 1) : 8 + (w >> 1), u = t < 4 ? (t & 1) : (w & 1);
          W[t][2 * ch + k2] = tr8(smem, 656, 32 * k2, (32 * blk + 8 * p4 + 4 * u) * 2, lane);
        }
      __syncthreads();
    }
  }
  const int nb = min(10, 576 - 10 * c);
  unsigned dst[3];
#pragma unroll
  for (int q = 0; q < 3; q++) {
    int blk = q < 2 ? 2 * w + q : 8 + (w >> 1);
    int gb = 10 * c + blk; int mat = gb / 192, bb = gb - mat * 192;
    dst[q] = blk < nb ? OFF_QF + mat * SZ_QF + bb * 2048 + lane * 16 + (q == 2 ? (w & 1) * 8 : 0) : OOB;
  }
  const int g0 = (10 * c) / 6;
  const int gid = g0 + lane; const int lo = max(10 * c, 6 * gid), hi = min(10 * c + nb, 6 * gid + 6);
  drain(); __syncthreads();
  TL(5, c == 0);
  for (int s = 0; s < NSEG; s++) {
    LAS char* xb = smem + (((s + 1) & 1) ? 81920 : 0);
    const int sn = s + 1 < NSEG ? s + 1 : s;
    const char* src = p.ws + OFF_XNF + sn * 40960 + w * 10240 + lane * 16;
    const unsigned ldst = lds0 + ((s & 1) ? 81920 : 0) + w * 10240;
    f32x4 acc[5][2];
    GST(0, s, 0, c == 0);
    dense_phase<5, 20>(acc, W, xb, lane, [&](int kk) { if (kk < 10) glds16(src + kk * 1024, ldst + kk * 1024); });
    GST(0, s, 1, c == 0);
    drain(); __syncthreads();
    GST(0, s, 2, c == 0);
    if (s > 4 && w == 0 && lane < 3 && hi > lo) atom_add(p.ws, ((s - 1) * 128 + gid) * 4, hi - lo);
#pragma unroll
    for (int tt = 0; tt < 2; tt++) {
      const unsigned so = s * 192 * 2048 + tt * 1024;
      __builtin_amdgcn_raw_buffer_store_b128(pack8(acc[0][tt], acc[1][tt]), ws, dst[0], so, 16);
      __builtin_amdgcn_raw_buffer_store_b128(pack8(acc[2][tt], acc[3][tt]), ws, dst[1], so, 16);
      __builtin_amdgcn_raw_buffer_store_b64((u32x2){pk2(acc[4][tt][0], acc[4][tt][1]), pk2(acc[4][tt][2], acc[4][tt][3])}, ws, dst[2], so, 16);
    }
    if (s < 4) { drain(); __syncthreads(); if (w == 0 && lane < 3 && hi > lo) atom_add(p.ws, (s * 128 + gid) * 4, hi - lo); }
  }
  drain(); __syncthreads();
  if (w == 0 && lane < 3 && hi > lo) atom_add(p.ws, ((NSEG - 1) * 128 + gid) * 4, hi - lo);
  TL(6, c == 0);
  __builtin_amdgcn_endpgm(); __builtin_unreachable();
}

DEVI void role_VO(const Params& p, const int wv, int h, LAS char* smem, rsrc_t ws) {
  int tid = wv * 64 + lane_id(); asm volatile("; roleVO" : "+v"(tid) :: "memory");
  const int lane = tid & 63, w = __builtin_amdgcn_readfirstlane(tid >> 6);
  bf16x8 W[10][6];
  {
    rsrc_t ro = __builtin_amdgcn_make_buffer_rsrc((void*)(p.Wo + (size_t)h * 192 * 640), 0, 192 * 640 * 2, 0x00020000);
    u32x4 pre[6][10];
    auto issue = [&](int ch) {
#pragma unroll
      for (int i = 0; i < 10; i++) pre[ch][i] = __builtin_amdgcn_raw_buffer_load_b128(ro, (i * 256 + tid) * 16, ch * 40960, 0);
    };
    issue(0); issue(1); issue(2); issue(3); issue(4); issue(5);
    const int p4 = lane & 3;
#pragma unroll
    for (int ch = 0; ch < 6; ch++) {
#pragma unroll
      for (int i = 0; i < 10; i++) { const int u = i * 256 + tid, row = u / 80, pc = u - row * 80; *(LAS u32x4*)(smem + row * 1328 + pc * 16) = pre[ch][i]; }
      __syncthreads();
#pragma unroll
      for (int t = 0; t < 10; t++) W[t][ch] = tr8(smem, 1328, 0, (16 * (10 * w + t) + 4 * p4) * 2, lane);
      __syncthreads();
    }
  }
  const int n = lane & 15, g = lane >> 4;
  const unsigned foff = lane == 0 ? (unsigned)((64 + h) * 4) : OOB;
  const unsigned ftgt = lane == 0 ? 6u : 0u;
  const unsigned zl = ((((20 * w + (n >> 3)) * 32 + h) * 5 + 1 + g) * 128 + (n & 7) * 16);
  u32x4 st[3];
  auto vload = [&](int s) {
#pragma unroll
    for (int i = 0; i < 3; i++) st[i] = ldx(ws, (i * 256 + tid) * 16, OFF_VF + (s * 192 + h * 6) * 2048);
  };
  auto vput = [&](int s) {
#pragma unroll
    for (int i = 0; i < 3; i++) *(LAS u32x4*)(smem + (s & 1) * 12288 + (i * 256 + tid) * 16) = st[i];
  };
  auto half = [&](f32x4 (&acc)[5][2], const bf16x8 (&d)[2][6], auto HF) {
    constexpr int hf = decltype(HF)::value;
#pragma unroll
    for (int ks = 0; ks < 6; ks++)
#pragma unroll
      for (int t = 0; t < 5; t++)
#pragma unroll
        for (int mt = 0; mt < 2; mt++) {
          if (ks == 0) mf0B<true>(acc[t][mt], d[mt][ks], W[5 * hf + t][ks]); else mfaB<true>(acc[t][mt], d[mt][ks], W[5 * hf + t][ks]);
        }
  };
  auto store = [&](f32x4 (&acc)[5][2], int s, int hf) {
#pragma unroll
    for (int t = 0; t < 5; t++) {
      asm volatile("" : "+v"(acc[t][0]), "+v"(acc[t][1]));
      __builtin_amdgcn_raw_buffer_store_b128(pack8(acc[t][0], acc[t][1]), ws, zl + (5 * hf + t) * 2 * 20480, OFF_Z + s * 80 * 20480, 16);
    }
  };
  wait_flags(ws, foff, ftgt);
  TL(8, h == 0);
  vload(0); vput(0);
  __syncthreads();
  for (int s = 0; s < NSEG; s++) {
    const int sn = s + 1 < NSEG ? s + 1 : s;
    const unsigned fo = foff == OOB ? OOB : foff + sn * 512;
    unsigned fl = ld1_sc1(ws, fo);
    bf16x8 cur[2][6];
#pragma unroll
    for (int ks = 0; ks < 6; ks++)
#pragma unroll
      for (int mt = 0; mt < 2; mt++) cur[mt][ks] = *(LAS bf16x8*)(smem + (s & 1) * 12288 + (ks * 2 + mt) * 1024 + lane * 16);
    SB();
    f32x4 acc0[5][2], acc1[5][2];
    GST(1, s, 0, h == 0);
    half(acc0, cur, std::integral_constant<int, 0>());
    SB();
    GST(1, s, 1, h == 0);
    drain(); __syncthreads();
    GST(1, s, 2, h == 0);
    if (s > 3 && tid == 0) atom_add(p.ws, OFF_REC + (s - 1) * 128 + 0, 1);
    const bool vready = __all(fl >= ftgt);
    if (vready) vload(sn);
    store(acc0, s, 0);
    SB();
    GST(1, s, 3, h == 0);
    half(acc1, cur, std::integral_constant<int, 1>());
    asm volatile("s_nop 7" ::: "memory");
    GST(1, s, 4, h == 0);
    store(acc1, s, 1);
    if (s < 3) { drain(); __syncthreads(); if (tid == 0) atom_add(p.ws, OFF_REC + s * 128 + 0, 1); }
    if (!vready) { wait_flags(ws, fo, ftgt); vload(sn); }
    vput(sn + (s + 1 < NSEG ? 0 : 1));
    __syncthreads();
  }
  drain(); __syncthreads();
  if (tid == 0) atom_add(p.ws, OFF_REC + (NSEG - 1) * 128 + 0, 1);
  TL(9, h == 0);
  __builtin_amdgcn_endpgm(); __builtin_unreachable();
}

DEVI void role_GU(const Params& p, const int wv, int c, LAS char* smem, rsrc_t ws) {
  int tid = wv * 64 + lane_id(); asm volatile("; roleGU" : "+v"(tid) :: "memory");
  const int lane = tid & 63, w = __builtin_amdgcn_readfirstlane(tid >> 6);
  const unsigned lds0 = (unsigned)(size_t)smem;
  bf16x8 W[4][20];
  x_dma(p, 0, lds0 + 81920, w, lane);
  {
    rsrc_t rg = __builtin_amdgcn_make_buffer_rsrc((void*)p.Wg, 0, 640 * 1536 * 2, 0x00020000);
    rsrc_t ru = __builtin_amdgcn_make_buffer_rsrc((void*)p.Wu, 0, 640 * 1536 * 2, 0x00020000);
    u32x4 pre[5][8];
    auto issue = [&](int ch) {
#pragma unroll
      for (int i = 0; i < 8; i++) { int u = i * 256 + tid; int row = (u >> 4) & 63, pc = u & 15;
        pre[ch % 5][i] = __builtin_amdgcn_raw_buffer_load_b128(i < 4 ? rg : ru, row * 3072 + pc * 16, ch * 64 * 3072 + c * 256, 0); }
    };
    issue(0); issue(1); issue(2); issue(3); issue(4);
    const int p4 = lane & 3;
#pragma unroll
    for (int ch = 0; ch < 10; ch++) {
#pragma unroll
      for (int i = 0; i < 8; i++) { const int u = i * 256 + tid; *(LAS u32x4*)(smem + (u >> 4) * 304 + (u & 15) * 16) = pre[ch % 5][i]; }
      __syncthreads();
      if (ch + 5 < 10) issue(ch + 5);
#pragma unroll
      for (int k2 = 0; k2 < 2; k2++)
#pragma unroll
        for (int t = 0; t < 4; t++) W[t][2 * ch + k2] = tr8(smem + (t >> 1) * (64 * 304), 304, 32 * k2, (32 * w + 8 * p4 + 4 * (t & 1)) * 2, lane);
      __syncthreads();
    }
  }
  drain(); __syncthreads();
  for (int s = 0; s < NSEG; s++) {
    LAS char* xb = smem + (((s + 1) & 1) ? 81920 : 0);
    const int sn = s + 1 < NSEG ? s + 1 : s;
    const char* src = p.ws + OFF_XNF + sn * 40960 + w * 10240 + lane * 16;
    const unsigned ldst = lds0 + ((s & 1) ? 81920 : 0) + w * 10240;
    f32x4 acc[4][2];
    dense_phase<4, 20>(acc, W, xb, lane, [&](int kk) { if (kk < 10) glds16(src + kk * 1024, ldst + kk * 1024); });
    drain(); __syncthreads();
    if (s > 0 && tid == 0) atom_add(p.ws, OFF_REC + (s - 1) * 128 + 4, 1);
#pragma unroll
    for (int tt = 0; tt < 2; tt++) {
      f32x4 h0, h1;
#pragma unroll
      for (int r = 0; r < 4; r++) { h0[r] = rbf(acc[0][tt][r]) * rbf(acc[2][tt][r]); h1[r] = rbf(acc[1][tt][r]) * rbf(acc[3][tt][r]); }
      __builtin_amdgcn_raw_buffer_store_b128(pack8(h0, h1), ws, lane * 16, OFF_HF + ((s * 48 + 4 * c + w) * 2 + tt) * 1024, 16);
    }
  }
  drain(); __syncthreads();
  if (tid == 0) atom_add(p.ws, OFF_REC + (NSEG - 1) * 128 + 4, 1);
  __builtin_amdgcn_endpgm(); __builtin_unreachable();
}

DEVI void role_DN(const Params& p, const int wv, int cc, LAS char* smem, rsrc_t ws) {
  const int c = cc % 5, par = cc / 5;
  int tid = wv * 64 + lane_id(); asm volatile("; roleDN" : "+v"(tid) :: "memory");
  const int lane = tid & 63, w = __builtin_amdgcn_readfirstlane(tid >> 6);
  bf16x8 W[2][48];
  {
    rsrc_t rd = __builtin_amdgcn_make_buffer_rsrc((void*)p.Wd, 0, 1536 * 640 * 2, 0x00020000);
    u32x4 pre[3][8];
    auto issue = [&](int ch) {
#pragma unroll
      for (int i = 0; i < 8; i++) { int u = i * 256 + tid; int row = u >> 4, pc = u & 15;
        pre[ch % 3][i] = __builtin_amdgcn_raw_buffer_load_b128(rd, row * 1280 + pc * 16, ch * 128 * 1280 + c * 256, 0); }
    };
    issue(0); issue(1); issue(2);
    const int p4 = lane & 3;
#pragma unroll
    for (int ch = 0; ch < 12; ch++) {
#pragma unroll
      for (int i = 0; i < 8; i++) { const int u = i * 256 + tid; *(LAS u32x4*)(smem + (u >> 4) * 304 + (u & 15) * 16) = pre[ch % 3][i]; }
      __syncthreads();
      if (ch + 3 < 12) issue(ch + 3);
#pragma unroll
      for (int k4 = 0; k4 < 4; k4++)
#pragma unroll
        for (int t = 0; t < 2; t++) W[t][4 * ch + k4] = tr8(smem, 304, 32 * k4, (32 * w + 8 * p4 + 4 * t) * 2, lane);
      __syncthreads();
    }
  }
  const unsigned foff = lane == 0 ? (unsigned)(OFF_REC + 4) : OOB;
  const unsigned ftgt = lane == 0 ? 12u : 0u;
  const int n = lane & 15, g = lane >> 4;
  u32x4 st[12];
  auto hload = [&](int s, int hf) {
#pragma unroll
    for (int i = 0; i < 12; i++) st[i] = __builtin_amdgcn_raw_buffer_load_b128(ws, (i * 256 + tid) * 16, OFF_HF + s * 98304 + hf * 49152, 16);
  };
  wait_flags(ws, foff == OOB ? OOB : foff + par * 128, ftgt);
  hload(par, 0);
  for (int s = par; s < NSEG; s += 2) {
    f32x4 acc[2][2];
#pragma unroll
    for (int hf = 0; hf < 2; hf++) {
#pragma unroll
      for (int i = 0; i < 12; i++) *(LAS u32x4*)(smem + hf * 49152 + (i * 256 + tid) * 16) = st[i];
      GST(2, s, 3 * hf, cc == 0);
      if (hf == 0) drain();
      __syncthreads();
      GST(2, s, 3 * hf + 1, cc == 0);
      if (hf == 0) { if (s > 1 && tid == 0) atom_add(p.ws, OFF_REC + (s - 2) * 128 + 8, 1); hload(s, 1); }
      else if (s + 2 < NSEG) { wait_flags(ws, foff == OOB ? OOB : foff + (s + 2) * 128, ftgt); hload(s + 2, 0); }
      GST(2, s, 3 * hf + 2, cc == 0);
      LAS char* xb = smem + hf * 49152;
      bf16x8 b[3][2];
#pragma unroll
      for (int q = 0; q < 2; q++) { b[q][0] = *(LAS bf16x8*)(xb + (q * 2) * 1024 + lane * 16); b[q][1] = *(LAS bf16x8*)(xb + (q * 2 + 1) * 1024 + lane * 16); }
#pragma unroll
      for (int kk = 0; kk < 24; kk++) {
        if (kk + 2 < 24) { b[(kk + 2) % 3][0] = *(LAS bf16x8*)(xb + ((kk + 2) * 2) * 1024 + lane * 16); b[(kk + 2) % 3][1] = *(LAS bf16x8*)(xb + ((kk + 2) * 2 + 1) * 1024 + lane * 16); }
        SB();
        const int kg = hf * 24 + kk;
#pragma unroll
        for (int t = 0; t < 2; t++)
#pragma unroll
          for (int tt = 0; tt < 2; tt++) {
            if (kg == 0) mf0<true>(acc[t][tt], W[t][kg], b[kk % 3][tt]);
            else if (t * 48 + kg < 64) mfa<true>(acc[t][tt], W[t][kg], b[kk % 3][tt]);
            else mfa<false>(acc[t][tt], W[t][kg], b[kk % 3][tt]);
          }
        SB();
      }
    }
    asm volatile("s_nop 7" ::: "memory");
#pragma unroll
    for (int tt = 0; tt < 2; tt++) {
      asm volatile("" : "+v"(acc[0][tt]), "+v"(acc[1][tt]));
      f32x4 g0, g1;
#pragma unroll
      for (int r = 0; r < 4; r++) {
        float d0 = rbf(acc[0][tt][r]), d1 = rbf(acc[1][tt][r]);
        g0[r] = d0 / (1.0f + __expf(-d0)); g1[r] = d1 / (1.0f + __expf(-d1));
      }
      const int tok = 32 * s + 8 * (n >> 2) + 4 * tt + (n & 3);
      __builtin_amdgcn_raw_buffer_store_b128(pack8(g0, g1), ws, OFF_G + tok * 1280 + (128 * c + 32 * w + 8 * g) * 2, 0, 16);
    }
  }
  drain(); __syncthreads();
  if (tid == 0) atom_add(p.ws, OFF_REC + (NSEG - 2 + par) * 128 + 8, 1);
  TL(10, cc == 0);
  __builtin_amdgcn_endpgm(); __builtin_unreachable();
}

DEVI void token_A(const Params& p, int s, int h, rsrc_t ws, int lane) {
  const int n = lane & 15, g = lane >> 4;
  wait_flags_slow(ws, lane == 0 ? (unsigned)((s * 128 + h) * 4) : lane == 1 ? (unsigned)((s * 128 + 32 + h) * 4) : OOB, lane < 2 ? 6u : 0u);
  bf16x8 qf[2][6], kf[2][6], km[6];
#pragma unroll
  for (int ks = 0; ks < 6; ks++)
#pragma unroll
    for (int tt = 0; tt < 2; tt++) {
      qf[tt][ks] = __builtin_bit_cast(bf16x8, ldx(ws, lane * 16, OFF_QF + ((s * 192 + h * 6 + ks) * 2 + tt) * 1024));
      kf[tt][ks] = __builtin_bit_cast(bf16x8, ldx(ws, lane * 16, OFF_KF + ((s * 192 + h * 6 + ks) * 2 + tt) * 1024));
    }
  SB();
  wait_flags_slow(ws, lane == 0 ? (unsigned)(OFF_KMF + (s * 32 + h) * 128) : OOB, lane == 0 ? 1u : 0u);
#pragma unroll
  for (int ks = 0; ks < 6; ks++) km[ks] = __builtin_bit_cast(bf16x8, ldx(ws, g * 128 + (n & 7) * 16, OFF_KM + ((s * 32 + h) * 6 + ks) * 512));
  const float cs = 0.07216878364870322f * 1.4426950408889634f;
#pragma unroll
  for (int tq = 0; tq < 2; tq++) {
    f32x4 T0 = {0, 0, 0, 0}, T1 = {0, 0, 0, 0}, T2 = {0, 0, 0, 0};
#pragma unroll
    for (int ks = 0; ks < 6; ks++) {
      T0 = __builtin_amdgcn_mfma_f32_16x16x32_bf16(km[ks], qf[tq][ks], T0, 0, 0, 0);
      T1 = __builtin_amdgcn_mfma_f32_16x16x32_bf16(kf[0][ks], qf[tq][ks], T1, 0, 0, 0);
      T2 = __builtin_amdgcn_mfma_f32_16x16x32_bf16(kf[1][ks], qf[tq][ks], T2, 0, 0, 0);
    }
    const int qt = 8 * (n >> 2) + 4 * tq + (n & 3);
    float m = fmaxf(fmaxf(T0[0], T0[1]), fmaxf(T0[2], T0[3]));
#pragma unroll
    for (int r = 0; r < 4; r++) {
      if (8 * g + r > qt) T1[r] = -3.0e38f;
      if (8 * g + 4 + r > qt) T2[r] = -3.0e38f;
      m = fmaxf(m, fmaxf(T1[r], T2[r]));
    }
    m = xor32max(xor16max(m));
    const float mc = m * cs;
    float l0 = 0, lx = 0; f32x4 E0, E1, E2;
#pragma unroll
    for (int r = 0; r < 4; r++) {
      E0[r] = __builtin_amdgcn_exp2f(__builtin_fmaf(T0[r], cs, -mc));
      E1[r] = (8 * g + r > qt) ? 0.f : __builtin_amdgcn_exp2f(__builtin_fmaf(T1[r], cs, -mc));
      E2[r] = (8 * g + 4 + r > qt) ? 0.f : __builtin_amdgcn_exp2f(__builtin_fmaf(T2[r], cs, -mc));
      l0 += E0[r]; lx += E1[r] + E2[r];
    }
    const float inv = 1.0f / (xor16add(l0) + xor32add(xor16add(lx)));
    E0 *= inv; E1 *= inv; E2 *= inv;
    const unsigned so = OFF_PX + (s * 160 + h * 5) * 512 + tq * 256;
    __builtin_amdgcn_raw_buffer_store_b64((u32x2){pk2(E0[0], E0[1]), pk2(E0[2], E0[3])}, ws, g < 2 ? (unsigned)(n * 16 + g * 8) : OOB, so, 16);
    __builtin_amdgcn_raw_buffer_store_b128(pack8(E1, E2), ws, (1 + g) * 512 + n * 16, so, 16);
  }
  drain();
  if (lane == 0) atom_add(p.ws, OFF_REC + s * 128 + 16, 1);
}

DEVI void token_B(const Params& p, int s, int cg, LAS char* smem, rsrc_t ws, int tid, int lane, int w) {
  const int n = lane & 15, g = lane >> 4;
  const unsigned zb = (16 * cg + 4 * w + (n >> 3)) * 20480 + (n & 7) * 16;
  const unsigned zx = zb + (1 + g) * 128, zy = zb + g * 640;
  const unsigned zs = OFF_Z + s * 80 * 20480;
  if (w == 0) wait_flags_slow(ws, lane < 2 ? (unsigned)(OFF_REC + s * 128 + (lane == 0 ? 0 : 8)) : OOB, lane == 0 ? 32u : lane == 1 ? 5u : 0u);
  __syncthreads();
  bf16x8 z[40][2];
#pragma unroll
  for (int q = 0; q < 28; q++)
#pragma unroll
    for (int u = 0; u < 2; u++) z[q][u] = __builtin_bit_cast(bf16x8, ldx(ws, zx + u * 2 * 20480 + q * 640, zs));
  u32x2 xv[2][2], gv[2][2];
#pragma unroll
  for (int u = 0; u < 2; u++)
#pragma unroll
    for (int tq = 0; tq < 2; tq++) {
      const int tok = 32 * s + 8 * (n >> 2) + 4 * tq + (n & 3);
      const int col = 128 * cg + 32 * w + 16 * u + 4 * g;
      xv[u][tq] = *(const u32x2*)(p.x + (size_t)tok * 640 + col);
      gv[u][tq] = __builtin_amdgcn_raw_buffer_load_b64(ws, OFF_G + tok * 1280 + col * 2, 0, 16);
    }
  SB();
  if (w == 0) {
    wait_flags_slow(ws, lane == 0 ? (unsigned)(OFF_REC + s * 128 + 16) : OOB, lane == 0 ? 32u : 0u);
    wait_flags_slow(ws, OFF_YF + s * 512 + lane * 4, 1u);
    wait_flags_slow(ws, lane < 16 ? (unsigned)(OFF_YF + s * 512 + 256 + lane * 4) : OOB, lane < 16 ? 1u : 0u);
  }
  __syncthreads();
  u32x4 st[20];
#pragma unroll
  for (int i = 0; i < 20; i++) st[i] = ldx(ws, (i * 256 + tid) * 16, OFF_PX + s * 81920);
#pragma unroll
  for (int q = 28; q < 32; q++)
#pragma unroll
    for (int u = 0; u < 2; u++) z[q][u] = __builtin_bit_cast(bf16x8, ldx(ws, zx + u * 2 * 20480 + q * 640, zs));
#pragma unroll
  for (int q = 32; q < 40; q++)
#pragma unroll
    for (int u = 0; u < 2; u++) z[q][u] = __builtin_bit_cast(bf16x8, ldx(ws, zy + u * 2 * 20480 + (q - 32) * 2560, zs));
  SB();
#pragma unroll
  for (int i = 0; i < 20; i++) *(LAS u32x4*)(smem + (i * 256 + tid) * 16) = st[i];
  __syncthreads();
  f32x4 acc[2][2] = {{{0, 0, 0, 0}, {0, 0, 0, 0}}, {{0, 0, 0, 0}, {0, 0, 0, 0}}};
#pragma unroll
  for (int ks = 0; ks < 40; ks++) {
    const int cb = ks < 32 ? (5 * ks + 1) * 512 + g * 512 : (20 * (ks - 32)) * 512 + g * 2560;
    bf16x8 b0 = *(LAS bf16x8*)(smem + cb + n * 16), b1 = *(LAS bf16x8*)(smem + cb + 256 + n * 16);
    acc[0][0] = __builtin_amdgcn_mfma_f32_16x16x32_bf16(z[ks][0], b0, acc[0][0], 0, 0, 0);
    acc[0][1] = __builtin_amdgcn_mfma_f32_16x16x32_bf16(z[ks][0], b1, acc[0][1], 0, 0, 0);
    acc[1][0] = __builtin_amdgcn_mfma_f32_16x16x32_bf16(z[ks][1], b0, acc[1][0], 0, 0, 0);
    acc[1][1] = __builtin_amdgcn_mfma_f32_16x16x32_bf16(z[ks][1], b1, acc[1][1], 0, 0, 0);
  }
#pragma unroll
  for (int u = 0; u < 2; u++)
#pragma unroll
    for (int tq = 0; tq < 2; tq++) {
      const int tok = 32 * s + 8 * (n >> 2) + 4 * tq + (n & 3);
      const int col = 128 * cg + 32 * w + 16 * u + 4 * g;
      float o[4];
#pragma unroll
      for (int r = 0; r < 4; r++) {
        unsigned xw = r < 2 ? xv[u][tq].x : xv[u][tq].y, gw = r < 2 ? gv[u][tq].x : gv[u][tq].y;
        float xf = __uint_as_float((r & 1) ? (xw & 0xffff0000u) : (xw << 16)), gf = __uint_as_float((r & 1) ? (gw & 0xffff0000u) : (gw << 16));
        o[r] = xf + rbf(rbf(acc[u][tq][r]) * gf);
      }
      *(u32x2*)(p.out + (size_t)tok * 640 + col) = (u32x2){pk2(o[0], o[1]), pk2(o[2], o[3])};
    }
  __syncthreads();
}

DEVI void role_TK(const Params& p, const int wv, int t, LAS char* smem, rsrc_t ws) {
  int tid = wv * 64 + lane_id(); asm volatile("; roleTK" : "+v"(tid) :: "memory");
  const int lane = tid & 63, w = __builtin_amdgcn_readfirstlane(tid >> 6);
  for (int idx = t; idx < (NSEG + 2) * 13; idx += N_TK) {
    const int sl = idx / 13, k = idx - sl * 13;
    GST(3, (idx / N_TK), 0, t == 0);
    if (k < 8) { if (sl < NSEG) token_A(p, sl, 4 * k + w, ws, lane); }
    else if (sl >= 2) token_B(p, sl - 2, k - 8, smem, ws, tid, lane, w);
    GST(3, (idx / N_TK), 1, t == 0);
  }
  TL(16 + t, true);
  __builtin_amdgcn_endpgm(); __builtin_unreachable();
}

constexpr int PB = 256;
DEVI void ln_row(const Params& p, int tok, u32x4 v0, u32x4 v1, int lane) {
  float f[16];
#pragma unroll
  for (int i = 0; i < 4; i++) {
    unsigned a = v0[i], c = v1[i];
    f[2 * i] = __uint_as_float(a << 16); f[2 * i + 1] = __uint_as_float(a & 0xffff0000u);
    f[8 + 2 * i] = __uint_as_float(c << 16); f[8 + 2 * i + 1] = __uint_as_float(c & 0xffff0000u);
  }
  float sum = 0;
#pragma unroll
  for (int i = 0; i < 16; i++) sum += f[i];
  for (int o = 32; o; o >>= 1) sum += __shfl_xor(sum, o);
  const float mean = sum * (1.0f / 640.0f);
  float sq = 0;
#pragma unroll
  for (int i = 0; i < 8; i++) { float d = f[i] - mean; sq += d * d; }
  if (lane < 16) {
#pragma unroll
    for (int i = 8; i < 16; i++) { float d = f[i] - mean; sq += d * d; }
  }
  for (int o = 32; o; o >>= 1) sq += __shfl_xor(sq, o);
  const float mb = rbf(mean), vb = rbf(sq * (1.0f / 640.0f));
  const float den = rbf(sqrtf(rbf(vb + 1e-5f)));
  float y[16];
#pragma unroll
  for (int i = 0; i < 16; i++) y[i] = rbf(f[i] - mb) / den;
  const int s = tok >> 5, t = tok & 31, tt = (t >> 2) & 1, n = 4 * (t >> 3) + (t & 3);
  int pc = lane; int cb = pc >> 2, g = pc & 3;
  *(u32x4*)(p.ws + OFF_XNF + ((s * 20 + cb) * 2 + tt) * 1024 + (n + 16 * g) * 16) = (u32x4){pk2(y[0], y[1]), pk2(y[2], y[3]), pk2(y[4], y[5]), pk2(y[6], y[7])};
  if (lane < 16) { pc = 64 + lane; cb = pc >> 2; g = pc & 3;
    *(u32x4*)(p.ws + OFF_XNF + ((s * 20 + cb) * 2 + tt) * 1024 + (n + 16 * g) * 16) = (u32x4){pk2(y[8], y[9]), pk2(y[10], y[11]), pk2(y[12], y[13]), pk2(y[14], y[15])}; }
}
__global__ __launch_bounds__(256) void k_init(Params p) {
  const int tid = threadIdx.x, lane = tid & 63, w = tid >> 6;
  const int b = blockIdx.x; const int wv = w;
  TL(11, b == 0);
  u32x4 xv[2][2];
#pragma unroll
  for (int r = 0; r < 2; r++) {
    const uint16_t* xr = p.x + (size_t)(b * 8 + w * 2 + r) * 640;
    xv[r][0] = *(const u32x4*)(xr + lane * 8); xv[r][1] = (u32x4){0, 0, 0, 0};
    if (lane < 16) xv[r][1] = *(const u32x4*)(xr + 512 + lane * 8);
  }
  const u32x4 ones = {SENT, SENT, SENT, SENT}, zero = {0, 0, 0, 0};
  for (unsigned o = OFF_MEMB + 10240 + (b * 256 + tid) * 16; o < SENT_END; o += PB * 256 * 16) *(u32x4*)(p.ws + o) = ones;
  for (unsigned o = (b * 256 + tid) * 16; o < FLAGS_END; o += PB * 256 * 16) *(u32x4*)(p.ws + o) = zero;
  if (b == 255) {
    for (int u = tid; u < 640; u += 256) {
      int j = u >> 3, i = u & 7;
      u32x4 v = *(const u32x4*)(p.mem0 + i * 640 + j * 8);
      v.x = san(v.x); v.y = san(v.y); v.z = san(v.z); v.w = san(v.w);
      *(u32x4*)(p.ws + OFF_MEMB + j * 128 + i * 16) = v;
    }
  }
#pragma unroll
  for (int r = 0; r < 2; r++) ln_row(p, b * 8 + w * 2 + r, xv[r][0], xv[r][1], lane);
  TL(13, b == 0);
}

DEVI void b_task(const Params& p, const int wv, int h, int nb, LAS char* sm, rsrc_t ws) {
  int tid = wv * 64 + lane_id(); asm volatile("; btask" : "+v"(tid) :: "memory");
  const int lane = tid & 63, w = wv;
  const int m = lane & 15, g = lane >> 4;
  u32x4 t15[15];
#pragma unroll
  for (int i = 0; i < 15; i++) { int u = i * 256 + tid; int row = u / 20, pc = u - row * 20; t15[i] = *(const u32x4*)(p.Wo + ((size_t)h * 192 + row) * 640 + 160 * nb + pc * 8); }
  bf16x8 an[4][6];
  rsrc_t rv = __builtin_amdgcn_make_buffer_rsrc((void*)p.Wv, 0, 640 * 6144 * 2, 0x00020000);
  const unsigned avo = m * 12288 + h * 384 + g * 16;
#pragma unroll
  for (int mt = 0; mt < 4; mt++)
#pragma unroll
    for (int ks = 0; ks < 6; ks++) an[mt][ks] = __builtin_bit_cast(bf16x8, __builtin_amdgcn_raw_buffer_load_b128(rv, avo + mt * (16 * 12288) + ks * 64, w * 64 * 12288, 0));
#pragma unroll
  for (int i = 0; i < 15; i++) { int u = i * 256 + tid; int row = u / 20, pc = u - row * 20; *(LAS u32x4*)(sm + row * 336 + pc * 16) = t15[i]; }
  __syncthreads();
  TL(60, h == 0 && nb == 0);
  bf16x8 Bf[10][6];
  const int p4 = lane & 3;
#pragma unroll
  for (int nt = 0; nt < 10; nt++)
#pragma unroll
    for (int ks = 0; ks < 6; ks++) Bf[nt][ks] = tr8(sm, 336, 32 * ks, (16 * nt + 4 * p4) * 2, lane);
  const unsigned bo = (((160 * nb + m) >> 3) * 16 + (h >> 1)) * 20480 + ((h & 1) * 8 + (m & 7) + 16 * g) * 16;
#pragma unroll 1
  for (int kk = w; kk < 10; kk += 4) {
    const int kn = kk + 4 < 10 ? kk + 4 : kk;
#pragma unroll
    for (int hf = 0; hf < 4; hf++) {
      const int u = hf & 1, c5 = (hf >> 1) * 5;
      f32x4 acc[2][5];
#pragma unroll
      for (int ks = 0; ks < 6; ks++)
#pragma unroll
        for (int a = 0; a < 2; a++)
#pragma unroll
          for (int nt = 0; nt < 5; nt++) acc[a][nt] = __builtin_amdgcn_mfma_f32_16x16x32_bf16(an[u + 2 * a][ks], Bf[c5 + nt][ks], ks == 0 ? (f32x4){0, 0, 0, 0} : acc[a][nt], 0, 0, 0);
#pragma unroll
      for (int nt = 0; nt < 5; nt++) {
        const f32x4 &A = acc[0][nt], &B = acc[1][nt];
        u32x4 v = {pk2(A[0], A[1]), pk2(B[0], B[1]), pk2(A[2], A[3]), pk2(B[2], B[3])};
        __builtin_amdgcn_raw_buffer_store_b128(v, ws, bo + (c5 + nt) * (2 * 16 * 20480) + u * 1024, OFF_BF + kk * 2048, 16);
      }
      if (hf >= 2) {
#pragma unroll
        for (int a = 0; a < 2; a++)
#pragma unroll
          for (int ks = 0; ks < 6; ks++) an[u + 2 * a][ks] = __builtin_bit_cast(bf16x8, __builtin_amdgcn_raw_buffer_load_b128(rv, avo + (u + 2 * a) * (16 * 12288) + ks * 64, kn * 64 * 12288, 0));
      }
      SB();
    }
  }
  TL(62, h == 0 && nb == 0);
  drain(); __syncthreads();
  if (tid == 0) atom_add(p.ws, OFF_BRDY + nb * 128, 1);
}

__global__ __launch_bounds__(256, 1) void k_main(Params p) {
  extern __shared__ __attribute__((aligned(16))) char smem_[];
  LAS char* smem = (LAS char*)smem_;
  rsrc_t ws = __builtin_amdgcn_make_buffer_rsrc(p.ws, 0, 0x7fffffff, 0x00020000);
  int b = blockIdx.x;
  const int wv = __builtin_amdgcn_readfirstlane(threadIdx.x >> 6);
  TL(0, b == 0);
  {
    constexpr int Q0 = N_S + N_V, NQ = 19, G0 = N_S + N_V + N_QKV + N_VO;
    const int x = b & 7;
    const int nq = (NQ - ((x - Q0) & 7) + 7) >> 3, ng = (256 - G0 - ((x - G0) & 7) + 7) >> 3;
    int rank = -1;
    if (b >= Q0 && b < Q0 + NQ) rank = (b - Q0) >> 3;
    else if (b >= G0) rank = nq + ((b - G0) >> 3);
    else if (b >= N_S && b < N_S + N_V) rank = nq + ng + ((b - N_S) >> 3);
    if (rank >= 0 && rank < 16 && (p.mask & 128)) b_task(p, wv, x + 8 * (rank & 3), rank >> 2, smem, ws);
  }
  TL(12, b == 0); TL(14, b == 40); TL(15, b == 120);
  if (b < N_S) { if (p.mask & 1) role_S(p, wv, b, smem, ws); return; }
  b -= N_S;
  if (b < N_V) { if (p.mask & 2) role_V(p, wv, b, smem, ws); return; }
  b -= N_V;
  if (b < N_QKV) { if (p.mask & 4) role_QKV(p, wv, b, smem, ws); return; }
  b -= N_QKV;
  if (b < N_VO) { if (p.mask & 8) role_VO(p, wv, b, smem, ws); return; }
  b -= N_VO;
  if (b < N_GU) { if (p.mask & 16) role_GU(p, wv, b, smem, ws); return; }
  b -= N_GU;
  if (b < N_DN) { if (p.mask & 32) role_DN(p, wv, b, smem, ws); return; }
  b -= N_DN;
  if (p.mask & 64) role_TK(p, wv, b, smem, ws);
}

static void launch_all(void* const* d_in, void* d_out, void* d_ws, const int* tune, const unsigned* masks, int nmasks, int do_pre, hipStream_t stream) {
  Params p;
  p.x = (const uint16_t*)d_in[0]; p.mem0 = (const uint16_t*)d_in[1];
  p.Wq = (const uint16_t*)d_in[2]; p.Wk = (const uint16_t*)d_in[3]; p.Wv = (const uint16_t*)d_in[4]; p.Wo = (const uint16_t*)d_in[5];
  p.Wg = (const uint16_t*)d_in[8]; p.Wu = (const uint16_t*)d_in[9]; p.Wd = (const uint16_t*)d_in[10];
  p.out = (uint16_t*)d_out; p.ws = (char*)d_ws;
  p.nsteps = NSEG; p.dS = tune[0]; p.dVm = tune[1]; p.dVe = tune[2]; p.dVl = tune[3]; p.inc = tune[4]; p.dec = tune[5]; p.mask = 0;
  (void)hipFuncSetAttribute((const void*)k_main, hipFuncAttributeMaxDynamicSharedMemorySize, 160 * 1024);
  if (do_pre) k_init<<<PB, 256, 0, stream>>>(p);
  for (int i = 0; i < nmasks; i++) { p.mask = masks[i]; k_main<<<256, 256, 160 * 1024, stream>>>(p); }
}
extern "C" void debug_launch(void* const* d_in, void* d_out, void* d_ws, const int* tune, const unsigned* masks, int nmasks, int do_pre, hipStream_t stream) {
  launch_all(d_in, d_out, d_ws, tune, masks, nmasks, do_pre, stream);
}
extern "C" void kernel_launch(void* const* d_in, const int* in_sizes, int n_in, void* d_out, int out_size, void* d_ws, size_t ws_size, hipStream_t stream) {
  const int tune[6] = {175, 175, 78, 110, 16, 1};
  const unsigned masks[1] = {255};
  launch_all(d_in, d_out, d_ws, tune, masks, 1, 1, stream);
}
```

```cpp
#include <hip/hip_runtime.h>
#include <stdint.h>
#include <type_traits>
typedef __attribute__((ext_vector_type(8), may_alias)) __bf16 bf16x8;
typedef __attribute__((ext_vector_type(16), may_alias)) __bf16 bf16x16;
typedef __attribute__((ext_vector_type(4), may_alias)) __bf16 bf16x4;
typedef __attribute__((ext_vector_type(2), may_alias)) __bf16 bf16x2;
typedef __attribute__((ext_vector_type(4), may_alias)) float f32x4;
typedef __attribute__((ext_vector_type(2), may_alias)) float f32x2;
typedef __attribute__((ext_vector_type(4), may_alias)) unsigned u32x4;
typedef __attribute__((ext_vector_type(2), may_alias)) unsigned u32x2;
typedef __amdgpu_buffer_rsrc_t rsrc_t;
#define LAS __attribute__((address_space(3)))
#define SENT 0xFFFFFFFFu
#define DEVI __device__ __forceinline__

constexpr int NSEG = 64;
constexpr unsigned OFF_FLAGS = 0;
constexpr unsigned OFF_MEMB = 1u << 20;
constexpr unsigned OFF_EBUF = OFF_MEMB + 768 * 1024;
constexpr unsigned OFF_PMB = OFF_EBUF + (1u << 20);
constexpr unsigned OFF_ALB = OFF_PMB + 256 * 1024;
constexpr unsigned OFF_TB = OFF_ALB + 64 * 1024;
constexpr unsigned SENT_END = OFF_TB + 32 * 1024;
constexpr unsigned OFF_XNF = 4u << 20;
constexpr unsigned OFF_QF = 8u << 20;
constexpr unsigned SZ_QF = 64u * 192 * 2 * 1024;
constexpr unsigned OFF_KF = OFF_QF + SZ_QF;
constexpr unsigned OFF_VF = OFF_QF + 2 * SZ_QF;
constexpr unsigned OFF_KM = OFF_QF + 3 * SZ_QF;
constexpr unsigned OFF_BF = OFF_KM + 64 * 32 * 6 * 512;
constexpr unsigned OFF_Z = OFF_BF + 80 * 16 * 10 * 2048;
constexpr unsigned OFF_PX = OFF_Z + 64u * 80 * 32 * 5 * 128;
constexpr unsigned OFF_HF = OFF_PX + 64 * 160 * 512;
constexpr unsigned OFF_G = OFF_HF + 64 * 96 * 1024;
constexpr unsigned OFF_TS = OFF_G + 2048 * 640 * 2;
constexpr unsigned OFF_REC = 32768;
constexpr unsigned OFF_BRDY = 49152;
constexpr unsigned OFF_TQ = 57344;
constexpr unsigned OFF_YF = 65536;
constexpr unsigned OFF_KMF = 131072;
constexpr unsigned FLAGS_END = OFF_KMF + 64 * 32 * 128;
constexpr int N_S = 32, N_V = 80, N_QKV = 58, N_VO = 32, N_GU = 12, N_DN = 10, N_TK = 32;

struct Params {
  const uint16_t* x; const uint16_t* mem0;
  const uint16_t* Wq; const uint16_t* Wk; const uint16_t* Wv; const uint16_t* Wo;
  const uint16_t* Wg; const uint16_t* Wu; const uint16_t* Wd;
  uint16_t* out;
  char* ws;
  int nsteps; int dS, dVm, dVe, dVl, inc, dec; unsigned mask;
};

DEVI int lane_id() { int l; asm volatile("v_mbcnt_lo_u32_b32 %0, -1, 0\n\tv_mbcnt_hi_u32_b32 %0, -1, %0" : "=v"(l)); return l; }
DEVI u32x4 ld_sc1(rsrc_t r, unsigned off) { return __builtin_amdgcn_raw_buffer_load_b128(r, off, 0, 16); }
DEVI unsigned ld1_sc1(rsrc_t r, unsigned off) { return __builtin_amdgcn_raw_buffer_load_b32(r, off, 0, 16); }
DEVI u32x4 ld_plain(rsrc_t r, unsigned off) { return __builtin_amdgcn_raw_buffer_load_b128(r, off, 0, 0); }
DEVI void st_sc1(rsrc_t r, unsigned off, u32x4 v) { __builtin_amdgcn_raw_buffer_store_b128(v, r, off, 0, 16); }
DEVI void st2_sc1(rsrc_t r, unsigned off, u32x2 v) { __builtin_amdgcn_raw_buffer_store_b64(v, r, off, 0, 16); }
DEVI void st1_sc1(rsrc_t r, unsigned off, unsigned v) { __builtin_amdgcn_raw_buffer_store_b32(v, r, off, 0, 16); }
DEVI unsigned mx4(u32x4 v) { return max(max(v.x, v.y), max(v.z, v.w)); }
DEVI unsigned pk2(float a, float b) { bf16x2 v = {(__bf16)a, (__bf16)b}; return __builtin_bit_cast(unsigned, v); }
DEVI unsigned san(unsigned v) { return min(v, 0xFFFFFFFEu); }
DEVI bf16x4 trread(LAS char* p) { return __builtin_amdgcn_ds_read_tr16_b64_v4bf16((LAS bf16x4*)p); }
DEVI float fmx(float a, float b) { float r; asm("v_max_f32 %0, %1, %2" : "=v"(r) : "v"(a), "v"(b)); return r; }
DEVI float fmx3(float a, float b, float c) { float r; asm("v_max3_f32 %0, %1, %2, %3" : "=v"(r) : "v"(a), "v"(b), "v"(c)); return r; }
template <bool AG> DEVI void smf(f32x4& acc, bf16x8 a, const bf16x16& b, int idx) {
  if constexpr (AG) asm volatile("v_smfmac_f32_16x16x64_bf16 %0, %1, %2, %3" : "+v"(acc) : "v"(a), "a"(b), "v"(idx));
  else asm volatile("v_smfmac_f32_16x16x64_bf16 %0, %1, %2, %3" : "+v"(acc) : "v"(a), "v"(b), "v"(idx));
}
DEVI f32x4 zero4() { float a, b, c, d; asm volatile("v_mov_b32 %0, 0" : "=v"(a)); asm volatile("v_mov_b32 %0, 0" : "=v"(b)); asm volatile("v_mov_b32 %0, 0" : "=v"(c)); asm volatile("v_mov_b32 %0, 0" : "=v"(d)); return (f32x4){a, b, c, d}; }
DEVI float ror8add(float x) { return x + __uint_as_float(__builtin_amdgcn_update_dpp(0u, __float_as_uint(x), 0x128, 0xf, 0xf, false)); }
DEVI float xor16max(float x) { auto r = __builtin_amdgcn_permlane16_swap(__float_as_uint(x), __float_as_uint(x), false, false); unsigned a = r[0], b = r[1]; return fmx(__uint_as_float(a), __uint_as_float(b)); }
DEVI float xor32max(float x) { auto r = __builtin_amdgcn_permlane32_swap(__float_as_uint(x), __float_as_uint(x), false, false); unsigned a = r[0], b = r[1]; return fmx(__uint_as_float(a), __uint_as_float(b)); }
DEVI float xor16add(float x) { auto r = __builtin_amdgcn_permlane16_swap(__float_as_uint(x), __float_as_uint(x), false, false); unsigned a = r[0], b = r[1]; return __uint_as_float(a) + __uint_as_float(b); }
DEVI float xor32add(float x) { auto r = __builtin_amdgcn_permlane32_swap(__float_as_uint(x), __float_as_uint(x), false, false); unsigned a = r[0], b = r[1]; return __uint_as_float(a) + __uint_as_float(b); }
DEVI f32x4 halfsum2(f32x4 A, f32x4 B) {
  f32x4 o;
#pragma unroll
  for (int r = 0; r < 4; r++) {
    float a = A[r], b = B[r];
    auto s = __builtin_amdgcn_permlane32_swap(__float_as_uint(a), __float_as_uint(b), false, false);
    unsigned s0 = s[0], s1 = s[1];
    o[r] = __uint_as_float(s0) + __uint_as_float(s1);
  }
  return o;
}
#define STAMP(role, k)
#define STAMP_DECL
#define STAMP_BEGIN
#define STAMP_FLUSH(role)
#define RT_DECL
#define RTS(k)
#define RTFAIL(bit, f)
#define RTF(cu)
#define TL(id, cond)
#define GST(role, seg, k, cond)
constexpr unsigned SPIN_CAP = 1u << 22;

DEVI unsigned now32() { unsigned long long t; asm volatile("s_memtime %0\n\ts_waitcnt lgkmcnt(0)" : "=s"(t) :: "memory"); return (unsigned)t; }
DEVI void wait_until(unsigned t) { while ((int)(now32() - t) < 0) { } }
DEVI unsigned rt32() { unsigned long long t; asm volatile("s_memrealtime %0\n\ts_waitcnt lgkmcnt(0)" : "=s"(t) :: "memory"); return (unsigned)t; }
DEVI void wait_rt(unsigned t) { while ((int)(rt32() - t) < 0) { } }
DEVI int wave_max32(int v) {
  const int lo = (int)0x80000000;
  v = max(v, __builtin_amdgcn_update_dpp(lo, v, 0x111, 0xf, 0xf, false));
  v = max(v, __builtin_amdgcn_update_dpp(lo, v, 0x112, 0xf, 0xf, false));
  v = max(v, __builtin_amdgcn_update_dpp(lo, v, 0x114, 0xf, 0xf, false));
  v = max(v, __builtin_amdgcn_update_dpp(lo, v, 0x118, 0xf, 0xf, false));
  v = max(v, __builtin_amdgcn_update_dpp(lo, v, 0x142, 0xa, 0xf, false));
  return __builtin_amdgcn_readlane(v, 31);
}
DEVI unsigned beacon_max(rsrc_t ws, int s, int lane, unsigned ref) {
  unsigned b = __builtin_amdgcn_raw_buffer_load_b32(ws, (lane & 31) * 16, OFF_TB + s * 512, 16);
  if (__builtin_amdgcn_ballot_w64(b == SENT) != 0) {
    for (unsigned spins = 0; spins < SPIN_CAP; spins++) {
      asm volatile("" ::: "memory");
      b = __builtin_amdgcn_raw_buffer_load_b32(ws, (lane & 31) * 16, OFF_TB + s * 512, 16);
      if (__builtin_amdgcn_ballot_w64(b == SENT) == 0) break;
    }
  }
  return ref + (unsigned)wave_max32((int)(b - ref));
}
DEVI void adapt(int& D, bool fail, int inc, int dec, int floor4) { D = fail ? D + inc : max(D - dec, floor4); }

DEVI u32x4 ldx(rsrc_t r, unsigned voff, unsigned soff) { return __builtin_amdgcn_raw_buffer_load_b128(r, voff, soff, 16); }
DEVI bool has_sent(unsigned m) { return __builtin_amdgcn_ballot_w64(m == SENT) != 0; }
#define SB() __builtin_amdgcn_sched_barrier(0)
DEVI bool gather_mem(rsrc_t ws, int s, LAS char* mlw, int w, unsigned voff) {
  const unsigned so = OFF_MEMB + s * 10240;
  bool fail = false;
  if (w < 2) {
    u32x4 v0 = ldx(ws, voff, so), v1 = ldx(ws, voff + 4096, so), v2 = ldx(ws, voff + 8192, so);
    SB();
    if (has_sent(max(max(mx4(v0), mx4(v1)), mx4(v2)))) {
      fail = true;
      for (unsigned spins = 0; spins < SPIN_CAP; spins++) {
        asm volatile("" ::: "memory");
        v0 = ldx(ws, voff, so); v1 = ldx(ws, voff + 4096, so); v2 = ldx(ws, voff + 8192, so);
        SB();
        if (!has_sent(max(max(mx4(v0), mx4(v1)), mx4(v2)))) break;
      }
    }
    *(LAS u32x4*)(mlw) = v0; *(LAS u32x4*)(mlw + 4096) = v1; *(LAS u32x4*)(mlw + 8192) = v2;
  } else {
    u32x4 v0 = ldx(ws, voff, so), v1 = ldx(ws, voff + 4096, so);
    SB();
    if (has_sent(max(mx4(v0), mx4(v1)))) {
      fail = true;
      for (unsigned spins = 0; spins < SPIN_CAP; spins++) {
        asm volatile("" ::: "memory");
        v0 = ldx(ws, voff, so); v1 = ldx(ws, voff + 4096, so);
        SB();
        if (!has_sent(max(mx4(v0), mx4(v1)))) break;
      }
    }
    *(LAS u32x4*)(mlw) = v0; *(LAS u32x4*)(mlw + 4096) = v1;
  }
  return fail;
}

DEVI bf16x16 cat16(bf16x8 lo, bf16x8 hi) { return __builtin_shufflevector(lo, hi, 0, 1, 2, 3, 4, 5, 6, 7, 8, 9, 10, 11, 12, 13, 14, 15); }

DEVI bf16x16 tr_frag(LAS char* tile, int ldb, int c0, int lane) {
  const int bg = lane >> 4, q4 = (lane & 15) >> 2, p4 = lane & 3;
  bf16x4 e4[4];
#pragma unroll
  for (int jj = 0; jj < 4; jj++) {
    int g = 2 * (jj >> 1) + (bg >> 1), pp = 2 * (bg & 1) + (jj & 1);
    int row = 8 * g + 2 * pp + (q4 & 1) + 32 * (q4 >> 1);
    e4[jj] = trread(tile + row * ldb + (row >> 5) * 32 + (c0 + 4 * p4) * 2);
  }
  bf16x16 f;
#pragma unroll
  for (int jj = 0; jj < 4; jj++)
#pragma unroll
    for (int e = 0; e < 4; e++) f[4 * jj + e] = e4[jj][e];
  return f;
}

struct AFr { bf16x8 a[10]; };
DEVI void a_load(AFr& A, LAS char* ml, int aoff) {
#pragma unroll
  for (int q = 0; q < 10; q++) A.a[q] = *(LAS bf16x8*)(ml + q * 1024 + aoff);
}
DEVI bf16x16 fb_load(LAS char* wl, int kk, int lane) { return cat16(*(LAS bf16x8*)(wl + kk * 2048 + lane * 16), *(LAS bf16x8*)(wl + kk * 2048 + 1024 + lane * 16)); }
template <int FI> DEVI void smfw(f32x4& acc, bf16x8 a, const bf16x16& b, int idx) { smf<(FI < 32)>(acc, a, b, idx); }
DEVI void acc_zero2(f32x4& a, f32x4& b) { a = (f32x4){0, 0, 0, 0}; b = (f32x4){0, 0, 0, 0}; }
DEVI void lds_signal(LAS char* c, int w, unsigned v) { asm volatile("" ::: "memory"); *(volatile LAS unsigned*)(c + w * 4) = v; asm volatile("" ::: "memory"); }
DEVI void lds_wait3(LAS char* c, unsigned target) {
  while (true) {
    asm volatile("" ::: "memory");
    u32x4 v = *(volatile LAS u32x4*)c;
    unsigned m = min(min(v.y, v.z), v.w);
    if (__builtin_amdgcn_readfirstlane(m) >= target) break;
  }
  asm volatile("" ::: "memory");
}
constexpr unsigned OOB = 0x80000000u;

DEVI void glds16(const void* gsrc, unsigned lds_dst) {
  unsigned keep;
  asm volatile("s_mov_b32 %0, m0\n\ts_mov_b32 m0, %2\n\ts_nop 0\n\tglobal_load_lds_dwordx4 %1, off\n\ts_mov_b32 m0, %0" : "=&s"(keep) : "v"(gsrc), "s"(lds_dst) : "memory");
}
DEVI void drain() { asm volatile("s_waitcnt vmcnt(0)" ::: "memory"); }
DEVI void atom_add(char* ws, unsigned off, unsigned v) { __hip_atomic_fetch_add((unsigned*)(ws + off), v, __ATOMIC_RELAXED, __HIP_MEMORY_SCOPE_AGENT); }
DEVI void wait_flags(rsrc_t ws, unsigned off, unsigned target) {
  unsigned spins = 0;
  while (true) {
    asm volatile("" ::: "memory");
    unsigned v = ld1_sc1(ws, off);
    if (__all(v >= target) || spins > SPIN_CAP) break;
    ++spins;
    __builtin_amdgcn_s_sleep(2);
  }
  asm volatile("" ::: "memory");
}

DEVI void role_S(const Params& p, const int wv, int h, LAS char* smem, rsrc_t ws) {
  const int w = wv;
  if (w == 0) {
    int tid = wv * 64 + lane_id(); asm volatile("; roleSA" : "+v"(tid) :: "memory");

  const int lane = tid & 63;
  const bool stampme = (h == 0);
  LAS char* wl = smem;
  LAS char* stg = smem + 122880;
  LAS char* ml = smem + 122880;
  LAS char* qk = ml + 10240;
  LAS char* kl = qk + 6144;
  LAS char* wlw = wl + (w - 1) * 40960;
  u32x4 pre[6][6];
  rsrc_t rq = __builtin_amdgcn_make_buffer_rsrc((void*)p.Wq, 0, 640 * 6144 * 2, 0x00020000);
  rsrc_t rk = __builtin_amdgcn_make_buffer_rsrc((void*)p.Wk, 0, 640 * 6144 * 2, 0x00020000);
  auto issue = [&](int it) {
    const int mat = it / 10, kk = it % 10;
#pragma unroll
    for (int i = 0; i < 6; i++) {
      int u = i * 256 + tid; int row = u / 24, pc = u - row * 24;
      pre[it % 6][i] = __builtin_amdgcn_raw_buffer_load_b128(mat ? rk : rq, row * 12288 + pc * 16, kk * 64 * 12288 + h * 384, 0);
    }
  };
  const int idx = (lane & 8) ? 0xEEEE : 0x4444;
  const int aoff = ((lane >> 3) & 1) * 512 + (lane >> 4) * 128 + (lane & 7) * 16;
  const float cs = 0.07216878364870322f * 1.4426950408889634f;
  int D = p.dS * 4;
  unsigned tref = 0;
    bf16x16 W[3][10];
    issue(0); issue(1); issue(2); issue(3); issue(4); issue(5);
#pragma unroll
    for (int it = 0; it < 20; it++) {
      const int mat = it / 10, kk = it % 10;
#pragma unroll
      for (int i = 0; i < 6; i++) { const int u = i * 256 + tid, row = u / 24, pc = u - row * 24; *(LAS u32x4*)(stg + row * 400 + (row >> 5) * 32 + pc * 16) = pre[it % 6][i]; }
      __syncthreads();
      if (it + 6 < 20) issue(it + 6);
      if (mat == 0) {
#pragma unroll
        for (int t = 0; t < 3; t++) W[t][kk] = tr_frag(stg, 400, 16 * t, lane);
      }
      __syncthreads();
    }
    const unsigned eoff = ((lane & 15) < 8) ? (unsigned)((lane >> 4) * 128 + (lane & 7) * 16) : OOB;
    const unsigned poff = ((lane & 15) < 8 && lane < 32) ? (unsigned)((lane & 7) * 16 + (lane >> 4) * 8) : OOB;
    const unsigned aloff = (lane < 8) ? (unsigned)(lane * 16) : OOB;
    TL(1, h == 0);
    u32x4 kx[12];
    const unsigned kfoff = lane == 0 ? (unsigned)((32 + h) * 4) : OOB, kftgt = lane == 0 ? 6u : 0u;
    wait_flags(ws, kfoff, kftgt);
#pragma unroll
    for (int i = 0; i < 12; i++) kx[i] = ld_sc1(ws, OFF_KF + (0 * 32 + h) * 12288 + i * 1024 + lane * 16);
    drain(); TL(2, h == 0);
    const int g = lane >> 4, q4 = (lane & 15) >> 2, p4 = lane & 3;
    LAS char* qw = qk + (lane & 15) * 32 + (lane >> 4) * 8;
    for (int s = 0; s < p.nsteps; s++) {
      STAMP_DECL STAMP_BEGIN STAMP(w, 0);
      RT_DECL
      if (s > 0) wait_rt(tref + (D >> 2));
      RTS(0);
      bool fail = gather_mem(ws, s, ml + w * 1024 + lane * 16, w, w * 1024 + lane * 16);
      RTS(1); RTFAIL(1, fail);
      if (s > 0) adapt(D, fail, p.inc, p.dec, p.dS * 4);
      STAMP(w, 1);
      __syncthreads();
      unsigned long long tb1;
      asm volatile("s_memrealtime %0" : "=s"(tb1));
      RTS(2);
      STAMP(w, 2);
      AFr A; a_load(A, ml, aoff);
      f32x4 a0 = {0, 0, 0, 0}, a1 = {0, 0, 0, 0}, a2 = {0, 0, 0, 0};
      asm volatile("s_nop 1" : "+v"(a0), "+v"(a1), "+v"(a2));
#pragma unroll
      for (int kk = 0; kk < 10; kk++) {
        smf<true>(a0, A.a[kk], W[0][kk], idx); smf<true>(a1, A.a[kk], W[1][kk], idx); smf<true>(a2, A.a[kk], W[2][kk], idx);
        if (kk == 4) { asm volatile("s_waitcnt lgkmcnt(0)" : "+s"(tb1)); __builtin_amdgcn_raw_buffer_store_b32(san((unsigned)tb1), ws, lane == 0 ? 0u : OOB, OFF_TB + s * 512 + h * 16, 16); }
      }
      asm volatile("s_nop 7" : "+v"(a0), "+v"(a1), "+v"(a2));
      STAMP(w, 3);
      *(LAS u32x2*)(qw) = (u32x2){pk2(a0[0], a0[1]), pk2(a0[2], a0[3])};
      *(LAS u32x2*)(qw + 512) = (u32x2){pk2(a1[0], a1[1]), pk2(a1[2], a1[3])};
      *(LAS u32x2*)(qw + 1024) = (u32x2){pk2(a2[0], a2[1]), pk2(a2[2], a2[3])};
      __syncthreads();
      STAMP(w, 4);
      bf16x8 qf[6];
#pragma unroll
      for (int ks = 0; ks < 6; ks++) {
        int e0 = 32 * ks + 8 * g + q4;
        bf16x4 qa = trread(qk + e0 * 32 + p4 * 8), qb = trread(qk + (e0 + 4) * 32 + p4 * 8);
        qf[ks] = __builtin_shufflevector(qa, qb, 0, 1, 2, 3, 4, 5, 6, 7);
      }
      f32x4 T1, T2;
      asm volatile("s_nop 1\n\tv_mfma_f32_16x16x32_bf16 %0, %2, %8, 0\n\tv_mfma_f32_16x16x32_bf16 %1, %3, %8, 0\n\t"
                   "v_mfma_f32_16x16x32_bf16 %0, %4, %9, %0\n\tv_mfma_f32_16x16x32_bf16 %1, %5, %9, %1\n\t"
                   "v_mfma_f32_16x16x32_bf16 %0, %6, %10, %0\n\tv_mfma_f32_16x16x32_bf16 %1, %7, %10, %1"
                   : "=&v"(T1), "=&v"(T2) : "v"(kx[0]), "v"(kx[1]), "v"(kx[2]), "v"(kx[3]), "v"(kx[4]), "v"(kx[5]), "v"(qf[0]), "v"(qf[1]), "v"(qf[2]));
      asm volatile("s_nop 1\n\tv_mfma_f32_16x16x32_bf16 %0, %2, %8, %0\n\tv_mfma_f32_16x16x32_bf16 %1, %3, %8, %1\n\t"
                   "v_mfma_f32_16x16x32_bf16 %0, %4, %9, %0\n\tv_mfma_f32_16x16x32_bf16 %1, %5, %9, %1\n\t"
                   "v_mfma_f32_16x16x32_bf16 %0, %6, %10, %0\n\tv_mfma_f32_16x16x32_bf16 %1, %7, %10, %1\n\ts_nop 7"
                   : "+v"(T1), "+v"(T2) : "v"(kx[6]), "v"(kx[7]), "v"(kx[8]), "v"(kx[9]), "v"(kx[10]), "v"(kx[11]), "v"(qf[3]), "v"(qf[4]), "v"(qf[5]));
      STAMP(w, 5);
#pragma unroll
      for (int r = 0; r < 4; r++) { T1[r] = ror8add(T1[r]); T2[r] = ror8add(T2[r]); }
      float mx = fmx3(fmx3(T1[0], T1[1], T1[2]), fmx3(T1[3], T2[0], T2[1]), fmx(T2[2], T2[3]));
      mx = xor32max(xor16max(mx));
      const float mxc = mx * cs;
      float E1[4], E2[4], lx = 0;
#pragma unroll
      for (int r = 0; r < 4; r++) { E1[r] = __builtin_fmaf(T1[r], cs, -mxc); E2[r] = __builtin_fmaf(T2[r], cs, -mxc); }
      SB();
#pragma unroll
      for (int r = 0; r < 4; r++) { E1[r] = __builtin_amdgcn_exp2f(E1[r]); E2[r] = __builtin_amdgcn_exp2f(E2[r]); }
      SB();
      {
        u32x4 ev = {san(pk2(E1[0], E1[1])), san(pk2(E1[2], E1[3])), san(pk2(E2[0], E2[1])), san(pk2(E2[2], E2[3]))};
        __builtin_amdgcn_raw_buffer_store_b128(ev, ws, eoff, OFF_EBUF + (s * 32 + h) * 512, 16);
      }
      RTS(3);
      STAMP(w, 6);
      lx = ((E1[0] + E1[1]) + (E1[2] + E1[3])) + ((E2[0] + E2[1]) + (E2[2] + E2[3]));
      lx = xor32add(xor16add(lx));
      __syncthreads();
      STAMP(w, 7);
      bf16x8 kmf[6];
#pragma unroll
      for (int ks = 0; ks < 6; ks++) {
        int e0 = 32 * ks + 8 * g + q4;
        bf16x4 ka = trread(kl + e0 * 16 + (p4 & 1) * 8), kb = trread(kl + (e0 + 4) * 16 + (p4 & 1) * 8);
        kmf[ks] = __builtin_shufflevector(ka, kb, 0, 1, 2, 3, 4, 5, 6, 7);
      }
      if (s == NSEG - 1) {
#pragma unroll
        for (int ks = 0; ks < 6; ks++) __builtin_amdgcn_raw_buffer_store_b128(__builtin_bit_cast(u32x4, kmf[ks]), ws, eoff, OFF_KM + ((s * 32 + h) * 6 + ks) * 512, 16);
        break;
      }
      f32x4 T0;
      asm volatile("s_nop 1\n\tv_mfma_f32_16x16x32_bf16 %0, %1, %4, 0\n\tv_mfma_f32_16x16x32_bf16 %0, %2, %5, %0\n\tv_mfma_f32_16x16x32_bf16 %0, %3, %6, %0"
                   : "=&v"(T0) : "v"(kmf[0]), "v"(kmf[1]), "v"(kmf[2]), "v"(qf[0]), "v"(qf[1]), "v"(qf[2]));
      asm volatile("s_nop 1\n\tv_mfma_f32_16x16x32_bf16 %0, %1, %4, %0\n\tv_mfma_f32_16x16x32_bf16 %0, %2, %5, %0\n\tv_mfma_f32_16x16x32_bf16 %0, %3, %6, %0\n\ts_nop 7"
                   : "+v"(T0) : "v"(kmf[3]), "v"(kmf[4]), "v"(kmf[5]), "v"(qf[3]), "v"(qf[4]), "v"(qf[5]));
#pragma unroll
      for (int r = 0; r < 4; r++) T0[r] = ror8add(T0[r]);
      float mm = xor16max(fmx(fmx3(T0[0], T0[1], T0[2]), T0[3]));
      float m = fmx(mx, mm);
      const float mc = m * cs;
      float ax = __builtin_amdgcn_exp2f(mxc - mc);
      float E0[4], lm;
#pragma unroll
      for (int r = 0; r < 4; r++) E0[r] = __builtin_amdgcn_exp2f(__builtin_fmaf(T0[r], cs, -mc));
      lm = xor16add((E0[0] + E0[1]) + (E0[2] + E0[3]));
      float inv = __builtin_amdgcn_rcpf(lx * ax + lm);
      {
        u32x2 pv = {san(pk2(E0[0] * inv, E0[1] * inv)), san(pk2(E0[2] * inv, E0[3] * inv))};
        __builtin_amdgcn_raw_buffer_store_b64(pv, ws, poff, OFF_PMB + (s * 32 + h) * 128, 16);
        __builtin_amdgcn_raw_buffer_store_b32(san(__float_as_uint(ax * inv)), ws, aloff, OFF_ALB + s * 1024 + ((h >> 3) * 2 + (h & 1)) * 128 + ((h >> 1) & 3) * 4, 16);
      }
      RTS(4);
      STAMP(w, 8);
#pragma unroll
      for (int ks = 0; ks < 6; ks++) __builtin_amdgcn_raw_buffer_store_b128(__builtin_bit_cast(u32x4, kmf[ks]), ws, eoff, OFF_KM + ((s * 32 + h) * 6 + ks) * 512, 16);
      if (s + 1 < NSEG) {
        wait_flags(ws, kfoff == OOB ? OOB : kfoff + (s + 1) * 512, kftgt);
        __builtin_amdgcn_raw_buffer_store_b32(1u, ws, lane == 0 ? 0u : OOB, OFF_KMF + (s * 32 + h) * 128, 16);
#pragma unroll
        for (int i = 0; i < 12; i++) kx[i] = ld_sc1(ws, OFF_KF + ((s + 1) * 32 + h) * 12288 + i * 1024 + lane * 16);
      }
      tref = beacon_max(ws, s, lane, (unsigned)tb1);
      RTF(h);
      STAMP_FLUSH(w);
      if (lane == 0 && s == p.nsteps - 1) { unsigned* d = (unsigned*)(p.ws + OFF_TS + 40960 + h * 16); d[0] = D; }
      if (tid == 0 && h == 0) { long long t = wall_clock64(); __builtin_amdgcn_raw_buffer_store_b64(__builtin_bit_cast(u32x2, t), ws, OFF_TS + s * 8, 0, 0); }
    }
    drain(); __builtin_amdgcn_raw_buffer_store_b32(1u, ws, lane == 0 ? 0u : OOB, OFF_KMF + ((p.nsteps - 1) * 32 + h) * 128, 16);
    __builtin_amdgcn_endpgm(); __builtin_unreachable();
  } else {
    int tid = wv * 64 + lane_id(); asm volatile("; roleSB" : "+v"(tid) :: "memory");

  const int lane = tid & 63;
  const bool stampme = (h == 0);
  LAS char* wl = smem;
  LAS char* stg = smem + 122880;
  LAS char* ml = smem + 122880;
  LAS char* qk = ml + 10240;
  LAS char* kl = qk + 6144;
  LAS char* wlw = wl + (w - 1) * 40960;
  u32x4 pre[6][6];
  rsrc_t rq = __builtin_amdgcn_make_buffer_rsrc((void*)p.Wq, 0, 640 * 6144 * 2, 0x00020000);
  rsrc_t rk = __builtin_amdgcn_make_buffer_rsrc((void*)p.Wk, 0, 640 * 6144 * 2, 0x00020000);
  auto issue = [&](int it) {
    const int mat = it / 10, kk = it % 10;
#pragma unroll
    for (int i = 0; i < 6; i++) {
      int u = i * 256 + tid; int row = u / 24, pc = u - row * 24;
      pre[it % 6][i] = __builtin_amdgcn_raw_buffer_load_b128(mat ? rk : rq, row * 12288 + pc * 16, kk * 64 * 12288 + h * 384, 0);
    }
  };
  const int idx = (lane & 8) ? 0xEEEE : 0x4444;
  const int aoff = ((lane >> 3) & 1) * 512 + (lane >> 4) * 128 + (lane & 7) * 16;
  const float cs = 0.07216878364870322f * 1.4426950408889634f;
  int D = p.dS * 4;
  unsigned tref = 0;
    LAS char* qw = qk + (48 * w + (lane & 15)) * 32 + (lane >> 4) * 8;
    bf16x16 W[5][10];
    issue(0); issue(1); issue(2); issue(3); issue(4); issue(5);
#pragma unroll
    for (int it = 0; it < 20; it++) {
      const int mat = it / 10, kk = it % 10;
#pragma unroll
      for (int i = 0; i < 6; i++) { const int u = i * 256 + tid, row = u / 24, pc = u - row * 24; *(LAS u32x4*)(stg + row * 400 + (row >> 5) * 32 + pc * 16) = pre[it % 6][i]; }
      __syncthreads();
      if (it + 6 < 20) issue(it + 6);
      const int nt = mat == 0 ? 3 : 4;
#pragma unroll
      for (int t = 0; t < 4; t++) if (t < nt) {
        bf16x16 f = tr_frag(stg, 400, mat == 0 ? 16 * (3 * w + t) : 16 * (4 * (w - 1) + t), lane);
        if (t < nt - 1) W[mat == 0 ? t : 2 + t][kk] = f;
        else { LAS char* d = wlw + (mat * 10 + kk) * 2048 + lane * 16;
          *(LAS bf16x8*)d = __builtin_shufflevector(f, f, 0, 1, 2, 3, 4, 5, 6, 7);
          *(LAS bf16x8*)(d + 1024) = __builtin_shufflevector(f, f, 8, 9, 10, 11, 12, 13, 14, 15); }
      }
      __syncthreads();
    }
    LAS char* k01addr = kl + (64 * (w - 1) + 16 * (lane >> 5) + (lane & 15)) * 16 + ((lane >> 4) & 1) * 8;
    for (int s = 0; s < p.nsteps; s++) {
      STAMP_DECL STAMP_BEGIN STAMP(w, 0);
      RT_DECL
      if (s > 0) wait_rt(tref + (D >> 2));
      RTS(0);
      bf16x16 fb[4];
#pragma unroll
      for (int q = 0; q < 3; q++) fb[q] = fb_load(wlw, q, lane);
      bool fail = gather_mem(ws, s, ml + w * 1024 + lane * 16, w, w * 1024 + lane * 16);
      RTS(1); RTFAIL(1, fail);
      if (s > 0) adapt(D, fail, p.inc, p.dec, p.dS * 4);
      STAMP(w, 1);
      __syncthreads();
      RTS(2);
      STAMP(w, 2);
      AFr A; a_load(A, ml, aoff);
      f32x4 a0 = {0, 0, 0, 0}, a1 = {0, 0, 0, 0}, a2 = {0, 0, 0, 0};
      asm volatile("s_nop 1" : "+v"(a0), "+v"(a1), "+v"(a2));
#pragma unroll
      for (int kk = 0; kk < 10; kk++) {
        smfw<0>(a0, A.a[kk], W[0][kk], idx);
        SB();
        bf16x8 lo = *(LAS bf16x8*)(wlw + (kk + 3) * 2048 + lane * 16);
        SB();
        if (10 + kk < 32) smf<true>(a1, A.a[kk], W[1][kk], idx); else smf<false>(a1, A.a[kk], W[1][kk], idx);
        SB();
        bf16x8 hi = *(LAS bf16x8*)(wlw + (kk + 3) * 2048 + 1024 + lane * 16);
        SB();
        smf<false>(a2, A.a[kk], fb[kk % 4], idx);
        fb[(kk + 3) % 4] = cat16(lo, hi);
        SB();
      }
      asm volatile("s_nop 7" : "+v"(a0), "+v"(a1), "+v"(a2));
      STAMP(w, 3);
      *(LAS u32x2*)(qw) = (u32x2){pk2(a0[0], a0[1]), pk2(a0[2], a0[3])};
      *(LAS u32x2*)(qw + 512) = (u32x2){pk2(a1[0], a1[1]), pk2(a1[2], a1[3])};
      *(LAS u32x2*)(qw + 1024) = (u32x2){pk2(a2[0], a2[1]), pk2(a2[2], a2[3])};
      SB();
      STAMP(w, 4);
      f32x4 y0 = {0, 0, 0, 0}, y1 = {0, 0, 0, 0}, x0 = {0, 0, 0, 0}, x1 = {0, 0, 0, 0};
      asm volatile("s_nop 1" : "+v"(y0), "+v"(y1), "+v"(x0), "+v"(x1));
#pragma unroll
      for (int kk = 0; kk < 10; kk++) {
        smf<false>(y0, A.a[kk], W[4][kk], idx);
        SB();
        bf16x8 lo, hi;
        if (kk + 3 < 10) lo = *(LAS bf16x8*)(wlw + (kk + 13) * 2048 + lane * 16);
        SB();
        smf<true>(x0, A.a[kk], W[2][kk], idx);
        SB();
        if (kk + 3 < 10) hi = *(LAS bf16x8*)(wlw + (kk + 13) * 2048 + 1024 + lane * 16);
        SB();
        if (kk < 2) smf<true>(x1, A.a[kk], W[3][kk], idx); else smf<false>(x1, A.a[kk], W[3][kk], idx);
        SB();
        smf<false>(y1, A.a[kk], fb[(kk + 10) % 4], idx);
        if (kk + 3 < 10) fb[(kk + 13) % 4] = cat16(lo, hi);
        SB();
        if (kk == 0) { asm volatile("s_waitcnt lgkmcnt(2)\n\ts_barrier" ::: "memory"); SB(); }
      }
      asm volatile("s_nop 7" : "+v"(x0), "+v"(x1), "+v"(y0), "+v"(y1));
      STAMP(w, 5);
      {
        f32x4 o = halfsum2(x0, x1), oy = halfsum2(y0, y1);
        *(LAS u32x2*)(k01addr) = (u32x2){pk2(o[0], o[1]), pk2(o[2], o[3])};
        *(LAS u32x2*)(k01addr + 512) = (u32x2){pk2(oy[0], oy[1]), pk2(oy[2], oy[3])};
      }
      __syncthreads();
      tref = beacon_max(ws, s, lane, rt32());
      RTF(h);
      STAMP(w, 7);
      STAMP_FLUSH(w);
    }
    __builtin_amdgcn_endpgm(); __builtin_unreachable();
  }
}

DEVI void role_V(const Params& p, const int wv, int j, LAS char* smem, rsrc_t ws) {
  int tid = wv * 64 + lane_id(); asm volatile("; roleV" : "+v"(tid) :: "memory");
  const int lane = tid & 63, w = __builtin_amdgcn_readfirstlane(tid >> 6);
  const bool stampme = (j == 0);
  LAS char* ml = smem;
  LAS char* yl = smem + 10240;
  LAS char* red = smem + 10240 + 4096;
  bf16x16 B[4][10];
  wait_flags(ws, lane == 0 ? (unsigned)(OFF_BRDY + (j / 20) * 128) : OOB, lane == 0 ? 32u : 0u);
#pragma unroll
  for (int t = 0; t < 4; t++)
#pragma unroll
    for (int kk = 0; kk < 10; kk++) {
      unsigned o = OFF_BF + ((j * 16 + 4 * w + t) * 10 + kk) * 2048 + lane * 16;
      B[t][kk] = cat16(__builtin_bit_cast(bf16x8, ld_sc1(ws, o)), __builtin_bit_cast(bf16x8, ld_sc1(ws, o + 1024)));
    }
  const int idx = (lane & 8) ? 0xEEEE : 0x4444;
  const int half = (lane >> 3) & 1, g = lane >> 4, i8 = lane & 7;
  const int aoff = half * 512 + g * 128 + i8 * 16;
  const int la = i8 + 16 * ((lane >> 3) & 1);
  const unsigned moff = ((lane & 15) < 8) ? (unsigned)(i8 * 16 + ((lane >> 4) & 1) * 8 + (lane >> 5) * 4) : OOB;
  const unsigned evo = (8 * w + half) * 512 + g * 128 + i8 * 16;
  const unsigned avo = (w * 2 + (g >> 1)) * 128 + i8 * 16;
  const unsigned pvo = (8 * w + 4 * half + g) * 128 + i8 * 16;
  const unsigned yvo = ((j * 32 + 8 * w + (lane >> 3)) * 5) * 128 + i8 * 16;
  const unsigned zvo = ((j * 32 + 8 * w + half) * 5 + 1 + g) * 128 + i8 * 16;
  const unsigned mso = j * 128;
  u32x4 zx[4];
  auto zload = [&](int s) {
#pragma unroll
    for (int q = 0; q < 4; q++) zx[q] = ldx(ws, zvo + q * 1280, OFF_Z + s * (80 * 20480));
  };
  drain(); TL(3, j == 0);
  const unsigned vfoff = lane == 0 ? (unsigned)(OFF_REC + 0) : OOB, vftgt = lane == 0 ? 32u : 0u;
  int Dm = p.dVm * 4, De = p.dVe * 4, Dl = p.dVl * 4;
  unsigned tb = 0;
  unsigned vfl = 0;
  for (int s = 0; s < p.nsteps; s++) {
    STAMP_DECL STAMP_BEGIN STAMP(4 + w, 0);
    RT_DECL
    if (s > 0) wait_rt(tb + (Dm >> 2));
    RTS(0);
    bool failm = gather_mem(ws, s, ml + w * 1024 + lane * 16, w, w * 1024 + lane * 16);
    RTS(1); RTFAIL(1, failm);
    if (!__all(vfl >= vftgt)) wait_flags(ws, vfoff == OOB ? OOB : vfoff + s * 128, vftgt);
    zload(s);
    if (s > 0) adapt(Dm, failm, p.inc, p.dec, p.dVm * 4);
    STAMP(4 + w, 1);
    __syncthreads();
    STAMP(4 + w, 2);
    f32x4 acc[4];
    {
      AFr A; a_load(A, ml, aoff);
#pragma unroll
      for (int t = 0; t < 4; t++) acc[t] = (f32x4){0, 0, 0, 0};
      asm volatile("s_nop 1" : "+v"(acc[0]), "+v"(acc[1]), "+v"(acc[2]), "+v"(acc[3]));
#pragma unroll
      for (int kk = 0; kk < 10; kk++)
#pragma unroll
        for (int t = 0; t < 4; t++) { if (t * 10 + kk < 32) smf<true>(acc[t], A.a[kk], B[t][kk], idx); else smf<false>(acc[t], A.a[kk], B[t][kk], idx); }
      asm volatile("s_nop 7" : "+v"(acc[0]), "+v"(acc[1]), "+v"(acc[2]), "+v"(acc[3]));
    }
#pragma unroll
    for (int pr = 0; pr < 2; pr++) {
      f32x4 o = halfsum2(acc[2 * pr], acc[2 * pr + 1]);
      int t = 2 * pr + (lane >> 5);
      int hl = 2 * t + ((lane >> 3) & 1);
      u32x2 v = {pk2(o[0], o[1]), pk2(o[2], o[3])};
      *(LAS u32x2*)(yl + w * 1024 + (hl * 8 + i8) * 16 + ((lane >> 4) & 1) * 8) = v;
    }
    bf16x8 yf = *(LAS bf16x8*)(yl + w * 1024 + ((4 * half + g) * 8 + i8) * 16);
    u32x4 yv = *(LAS u32x4*)(yl + w * 1024 + lane * 16);
    if (s == NSEG - 1) {
      __builtin_amdgcn_raw_buffer_store_b128(yv, ws, yvo, OFF_Z + s * (80 * 20480), 16);
      drain(); __syncthreads();
      if (w == 1) __builtin_amdgcn_raw_buffer_store_b32(1u, ws, lane == 0 ? (unsigned)(j * 4) : OOB, OFF_YF + s * 512, 16);
      break;
    }
    STAMP(4 + w, 3);
    f32x4 ox[4]; bool znext = false, fe_ = false, fl_ = false;
    {
      u32x4 ev[4]; u32x4 al; u32x4 pm;
      const unsigned se = OFF_EBUF + s * 16384, sa = OFF_ALB + s * 1024, sp = OFF_PMB + s * 4096;
      auto ld_e = [&]() {
#pragma unroll
        for (int q = 0; q < 4; q++) ev[q] = ldx(ws, evo + q * 1024, se);
      };
      auto ld_l = [&]() { al = ldx(ws, avo, sa); pm = ldx(ws, pvo, sp); };
      tb = beacon_max(ws, s, lane, rt32());
      __builtin_amdgcn_raw_buffer_store_b128(yv, ws, yvo, OFF_Z + s * (80 * 20480), 16);
      const unsigned te = tb + (De >> 2), tl = tb + (Dl >> 2);
      SB();
      wait_rt(te);
      RTS(2);
      ld_e();
      vfl = ld1_sc1(ws, (vfoff == OOB || s + 1 >= NSEG) ? OOB : vfoff + (s + 1) * 128);
      SB();
      wait_rt(tl);
      RTS(3);
      ld_l();
      SB();
      bool fe = false, fl = false;
      if (has_sent(max(max(mx4(ev[0]), mx4(ev[1])), max(mx4(ev[2]), mx4(ev[3]))))) {
        fe = true;
        for (unsigned spins = 0; spins < SPIN_CAP; spins++) {
          asm volatile("" ::: "memory");
          ld_e();
          SB();
          if (!has_sent(max(max(mx4(ev[0]), mx4(ev[1])), max(mx4(ev[2]), mx4(ev[3]))))) break;
        }
      }
      asm volatile("s_nop 1\n\tv_mfma_f32_16x16x32_bf16 %0, %4, %8, 0\n\tv_mfma_f32_16x16x32_bf16 %1, %5, %9, 0\n\tv_mfma_f32_16x16x32_bf16 %2, %6, %10, 0\n\tv_mfma_f32_16x16x32_bf16 %3, %7, %11, 0"
                   : "=&v"(ox[0]), "=&v"(ox[1]), "=&v"(ox[2]), "=&v"(ox[3]) : "v"(zx[0]), "v"(zx[1]), "v"(zx[2]), "v"(zx[3]), "v"(ev[0]), "v"(ev[1]), "v"(ev[2]), "v"(ev[3]));
      STAMP(4 + w, 4);
      SB();
      float s01, s23;
      auto late = [&]() {
        f32x4 tm4;
        asm volatile("s_nop 1\n\tv_mfma_f32_16x16x32_bf16 %0, %1, %2, 0" : "=&v"(tm4) : "v"(yf), "v"(pm));
        asm volatile("s_nop 3" : "+v"(ox[0]), "+v"(ox[1]), "+v"(ox[2]), "+v"(ox[3]));
        const float a0 = __uint_as_float(al.x), a1 = __uint_as_float(al.y), a2 = __uint_as_float(al.z), a3 = __uint_as_float(al.w);
        f32x4 u0 = ox[0] * a0, u1 = ox[2] * a2;
        u0 = __builtin_elementwise_fma(ox[1], (f32x4){a1, a1, a1, a1}, u0); u1 = __builtin_elementwise_fma(ox[3], (f32x4){a3, a3, a3, a3}, u1);
        u0 += u1;
        asm volatile("" : "+v"(tm4), "+v"(u0));
        u0 += tm4;
        float t[4] = {u0[0], u0[1], u0[2], u0[3]};
        auto a = __builtin_amdgcn_permlane32_swap(__float_as_uint(t[0]), __float_as_uint(t[1]), false, false);
        auto b = __builtin_amdgcn_permlane32_swap(__float_as_uint(t[2]), __float_as_uint(t[3]), false, false);
        unsigned a0_ = a[0], a1_ = a[1], b0_ = b[0], b1_ = b[1];
        s01 = __uint_as_float(a0_) + __uint_as_float(__builtin_amdgcn_update_dpp(0u, a1_, 0x128, 0xf, 0xf, false));
        s23 = __uint_as_float(b0_) + __uint_as_float(__builtin_amdgcn_update_dpp(0u, b1_, 0x128, 0xf, 0xf, false));
      };
      late();
      if (has_sent(max(mx4(al), mx4(pm)))) {
        fl = true;
        for (unsigned spins = 0; spins < SPIN_CAP; spins++) {
          asm volatile("" ::: "memory");
          ld_l();
          SB();
          if (!has_sent(max(mx4(al), mx4(pm)))) break;
        }
        late();
      }
      RTS(4); RTFAIL(2, fe); RTFAIL(4, fl);
      znext = s + 1 < NSEG; fe_ = fe; fl_ = fl;
      STAMP(4 + w, 5);
      if (w != 0) *(LAS f32x2*)(red + w * 512 + lane * 8) = (f32x2){s01, s23};
      __syncthreads();
      if (w == 1) __builtin_amdgcn_raw_buffer_store_b32(1u, ws, lane == 0 ? (unsigned)(j * 4) : OOB, OFF_YF + s * 512, 16);
      STAMP(4 + w, 6);
      if (w == 0) {
        f32x2 r1 = *(LAS f32x2*)(red + 512 + lane * 8), r2 = *(LAS f32x2*)(red + 1024 + lane * 8), r3 = *(LAS f32x2*)(red + 1536 + lane * 8);
        const float u01 = (s01 + r1[0]) + (r2[0] + r3[0]), u23 = (s23 + r1[1]) + (r2[1] + r3[1]);
        auto c = __builtin_amdgcn_permlane32_swap(__float_as_uint(u01), __float_as_uint(u23), false, false);
        unsigned c0 = c[0], c1 = c[1];
        __builtin_amdgcn_raw_buffer_store_b32(san(pk2(__uint_as_float(c0), __uint_as_float(c1))), ws, moff, OFF_MEMB + (s + 1) * 10240 + j * 128, 16);
      }
    }
    RTS(5);
    adapt(De, fe_, p.inc, p.dec, p.dVe * 4); adapt(Dl, fl_, p.inc, p.dec, p.dVl * 4);
    RTF(32 + j);
    STAMP(4 + w, 7);
    STAMP_FLUSH(4 + w);
    if (lane == 0 && s == p.nsteps - 1) { unsigned* d = (unsigned*)(p.ws + OFF_TS + 32768 + (j * 4 + w) * 16); d[0] = Dm; d[1] = De; d[2] = Dl; }
  }
  __builtin_amdgcn_endpgm(); __builtin_unreachable();
}

template <bool AG> DEVI void mf0(f32x4& acc, const bf16x8& a, const bf16x8& b) {
  if constexpr (AG) asm volatile("v_mfma_f32_16x16x32_bf16 %0, %1, %2, 0" : "=&v"(acc) : "a"(a), "v"(b));
  else asm volatile("v_mfma_f32_16x16x32_bf16 %0, %1, %2, 0" : "=&v"(acc) : "v"(a), "v"(b));
}
template <bool AG> DEVI void mfa(f32x4& acc, const bf16x8& a, const bf16x8& b) {
  if constexpr (AG) asm volatile("v_mfma_f32_16x16x32_bf16 %0, %1, %2, %0" : "+v"(acc) : "a"(a), "v"(b));
  else asm volatile("v_mfma_f32_16x16x32_bf16 %0, %1, %2, %0" : "+v"(acc) : "v"(a), "v"(b));
}
template <bool AG> DEVI void mf0B(f32x4& acc, const bf16x8& a, const bf16x8& b) {
  if constexpr (AG) asm volatile("v_mfma_f32_16x16x32_bf16 %0, %1, %2, 0" : "=&v"(acc) : "v"(a), "a"(b));
  else asm volatile("v_mfma_f32_16x16x32_bf16 %0, %1, %2, 0" : "=&v"(acc) : "v"(a), "v"(b));
}
template <bool AG> DEVI void mfaB(f32x4& acc, const bf16x8& a, const bf16x8& b) {
  if constexpr (AG) asm volatile("v_mfma_f32_16x16x32_bf16 %0, %1, %2, %0" : "+v"(acc) : "v"(a), "a"(b));
  else asm volatile("v_mfma_f32_16x16x32_bf16 %0, %1, %2, %0" : "+v"(acc) : "v"(a), "v"(b));
}
DEVI void wait_flags_slow(rsrc_t ws, unsigned off, unsigned target, bool fast = false) {
  unsigned spins = 0;
  while (true) {
    asm volatile("" ::: "memory");
    unsigned v = ld1_sc1(ws, off);
    if (__all(v >= target) || spins > SPIN_CAP) break;
    ++spins;
    if (fast) __builtin_amdgcn_s_sleep(2); else __builtin_amdgcn_s_sleep(12);
  }
  asm volatile("" ::: "memory");
}
DEVI bf16x8 tr8(LAS char* tile, int ldb, int row0, int colbyte, int lane) {
  const int g = lane >> 4, q4 = (lane & 15) >> 2;
  bf16x4 a = trread(tile + (row0 + 8 * g + q4) * ldb + colbyte), b = trread(tile + (row0 + 8 * g + 4 + q4) * ldb + colbyte);
  return __builtin_shufflevector(a, b, 0, 1, 2, 3, 4, 5, 6, 7);
}
DEVI u32x4 pack8(const f32x4& a, const f32x4& b) { return (u32x4){pk2(a[0], a[1]), pk2(a[2], a[3]), pk2(b[0], b[1]), pk2(b[2], b[3])}; }
DEVI float bf2f(unsigned short v) { return __uint_as_float((unsigned)v << 16); }
DEVI float rbf(float v) { return (float)(__bf16)v; }

DEVI void x_dma(const Params& p, int s, unsigned ldsbase, int w, int lane) {
  const char* src = p.ws + OFF_XNF + s * 40960 + w * 10240 + lane * 16;
#pragma unroll
  for (int i = 0; i < 10; i++) glds16(src + i * 1024, ldsbase + w * 10240 + i * 1024);
}

template <int NT, int NKS, class F>
DEVI void dense_phase(f32x4 (&acc)[NT][2], const bf16x8 (&W)[NT][NKS], LAS char* xb, int lane, F filler) {
  bf16x8 b[3][2];
#pragma unroll
  for (int q = 0; q < 2; q++) { b[q][0] = *(LAS bf16x8*)(xb + (q * 2) * 1024 + lane * 16); b[q][1] = *(LAS bf16x8*)(xb + (q * 2 + 1) * 1024 + lane * 16); }
#pragma unroll
  for (int kk = 0; kk < NKS; kk++) {
#pragma unroll
    for (int t = 0; t < NT; t++)
#pragma unroll
      for (int tt = 0; tt < 2; tt++) {
        if (kk == 0) { if (t * NKS + kk < 64) mf0<true>(acc[t][tt], W[t][kk], b[kk % 3][tt]); else mf0<false>(acc[t][tt], W[t][kk], b[kk % 3][tt]); }
        else { if (t * NKS + kk < 64) mfa<true>(acc[t][tt], W[t][kk], b[kk % 3][tt]); else mfa<false>(acc[t][tt], W[t][kk], b[kk % 3][tt]); }
        SB();
        if (t == 0 && tt == 1 && kk + 2 < NKS) b[(kk + 2) % 3][0] = *(LAS bf16x8*)(xb + ((kk + 2) * 2) * 1024 + lane * 16);
        if (t == 1 && tt == 1 && kk + 2 < NKS) b[(kk + 2) % 3][1] = *(LAS bf16x8*)(xb + ((kk + 2) * 2 + 1) * 1024 + lane * 16);
        if (t == 2 && tt == 1) filler(kk);
        SB();
      }
  }
  asm volatile("s_nop 7" ::: "memory");
#pragma unroll
  for (int t = 0; t < NT; t++) asm volatile("" : "+v"(acc[t][0]), "+v"(acc[t][1]));
}

DEVI void role_QKV(const Params& p, const int wv, int c, LAS char* smem, rsrc_t ws) {
  int tid = wv * 64 + lane_id(); asm volatile("; roleQKV" : "+v"(tid) :: "memory");
  const int lane = tid & 63, w = __builtin_amdgcn_readfirstlane(tid >> 6);
  const unsigned lds0 = (unsigned)(size_t)smem;
  bf16x8 W[5][20];
  x_dma(p, 0, lds0 + 81920, w, lane);
  {
    const int pc = tid % 40, r0 = min(tid / 40, 5);
    const int gcol = min(320 * c + 8 * pc, 18432 - 8);
    const int mat = gcol / 6144, cm = gcol - mat * 6144;
    const uint16_t* base = (mat == 0 ? p.Wq : mat == 1 ? p.Wk : p.Wv) + cm;
    u32x4 pre[3][11];
    auto issue = [&](int ch) {
#pragma unroll
      for (int i = 0; i < 11; i++) { int r = min(r0 + 6 * i, 63); pre[ch % 3][i] = *(const u32x4*)(base + (size_t)(64 * ch + r) * 6144); }
    };
    issue(0); issue(1); issue(2);
    const int p4 = lane & 3;
#pragma unroll
    for (int ch = 0; ch < 10; ch++) {
#pragma unroll
      for (int i = 0; i < 11; i++) { int r = min(r0 + 6 * i, 63); *(LAS u32x4*)(smem + r * 656 + pc * 16) = pre[ch % 3][i]; }
      __syncthreads();
      if (ch + 3 < 10) issue(ch + 3);
#pragma unroll
      for (int k2 = 0; k2 < 2; k2++)
#pragma unroll
        for (int t = 0; t < 5; t++) {
          const int blk = t < 4 ? 2 * w + (t >> 1) : 8 + (w >> 1), u = t < 4 ? (t & 1) : (w & 1);
          W[t][2 * ch + k2] = tr8(smem, 656, 32 * k2, (32 * blk + 8 * p4 + 4 * u) * 2, lane);
        }
      __syncthreads();
    }
  }
  const int nb = min(10, 576 - 10 * c);
  unsigned dst[3];
#pragma unroll
  for (int q = 0; q < 3; q++) {
    int blk = q < 2 ? 2 * w + q : 8 + (w >> 1);
    int gb = 10 * c + blk; int mat = gb / 192, bb = gb - mat * 192;
    dst[q] = blk < nb ? OFF_QF + mat * SZ_QF + bb * 2048 + lane * 16 + (q == 2 ? (w & 1) * 8 : 0) : OOB;
  }
  const int g0 = (10 * c) / 6;
  const int gid = g0 + lane; const int lo = max(10 * c, 6 * gid), hi = min(10 * c + nb, 6 * gid + 6);
  drain(); __syncthreads();
  TL(5, c == 0);
  for (int s = 0; s < NSEG; s++) {
    LAS char* xb = smem + (((s + 1) & 1) ? 81920 : 0);
    const int sn = s + 1 < NSEG ? s + 1 : s;
    const char* src = p.ws + OFF_XNF + sn * 40960 + w * 10240 + lane * 16;
    const unsigned ldst = lds0 + ((s & 1) ? 81920 : 0) + w * 10240;
    f32x4 acc[5][2];
    GST(0, s, 0, c == 0);
    dense_phase<5, 20>(acc, W, xb, lane, [&](int kk) { if (kk < 10) glds16(src + kk * 1024, ldst + kk * 1024); });
    GST(0, s, 1, c == 0);
    drain(); __syncthreads();
    GST(0, s, 2, c == 0);
    if (s > 4 && w == 0 && lane < 3 && hi > lo) atom_add(p.ws, ((s - 1) * 128 + gid) * 4, hi - lo);
#pragma unroll
    for (int tt = 0; tt < 2; tt++) {
      const unsigned so = s * 192 * 2048 + tt * 1024;
      __builtin_amdgcn_raw_buffer_store_b128(pack8(acc[0][tt], acc[1][tt]), ws, dst[0], so, 16);
      __builtin_amdgcn_raw_buffer_store_b128(pack8(acc[2][tt], acc[3][tt]), ws, dst[1], so, 16);
      __builtin_amdgcn_raw_buffer_store_b64((u32x2){pk2(acc[4][tt][0], acc[4][tt][1]), pk2(acc[4][tt][2], acc[4][tt][3])}, ws, dst[2], so, 16);
    }
    if (s < 4) { drain(); __syncthreads(); if (w == 0 && lane < 3 && hi > lo) atom_add(p.ws, (s * 128 + gid) * 4, hi - lo); }
  }
  drain(); __syncthreads();
  if (w == 0 && lane < 3 && hi > lo) atom_add(p.ws, ((NSEG - 1) * 128 + gid) * 4, hi - lo);
  TL(6, c == 0);

}

DEVI void role_VO(const Params& p, const int wv, int h, LAS char* smem, rsrc_t ws) {
  int tid = wv * 64 + lane_id(); asm volatile("; roleVO" : "+v"(tid) :: "memory");
  const int lane = tid & 63, w = __builtin_amdgcn_readfirstlane(tid >> 6);
  bf16x8 W[10][6];
  {
    rsrc_t ro = __builtin_amdgcn_make_buffer_rsrc((void*)(p.Wo + (size_t)h * 192 * 640), 0, 192 * 640 * 2, 0x00020000);
    u32x4 pre[6][10];
    auto issue = [&](int ch) {
#pragma unroll
      for (int i = 0; i < 10; i++) pre[ch][i] = __builtin_amdgcn_raw_buffer_load_b128(ro, (i * 256 + tid) * 16, ch * 40960, 0);
    };
    issue(0); issue(1); issue(2); issue(3); issue(4); issue(5);
    const int p4 = lane & 3;
#pragma unroll
    for (int ch = 0; ch < 6; ch++) {
#pragma unroll
      for (int i = 0; i < 10; i++) { const int u = i * 256 + tid, row = u / 80, pc = u - row * 80; *(LAS u32x4*)(smem + row * 1328 + pc * 16) = pre[ch][i]; }
      __syncthreads();
#pragma unroll
      for (int t = 0; t < 10; t++) W[t][ch] = tr8(smem, 1328, 0, (16 * (10 * w + t) + 4 * p4) * 2, lane);
      __syncthreads();
    }
  }
  const int n = lane & 15, g = lane >> 4;
  const unsigned foff = lane == 0 ? (unsigned)((64 + h) * 4) : OOB;
  const unsigned ftgt = lane == 0 ? 6u : 0u;
  const unsigned zl = ((((20 * w + (n >> 3)) * 32 + h) * 5 + 1 + g) * 128 + (n & 7) * 16);
  u32x4 st[3];
  auto vload = [&](int s) {
#pragma unroll
    for (int i = 0; i < 3; i++) st[i] = ldx(ws, (i * 256 + tid) * 16, OFF_VF + (s * 192 + h * 6) * 2048);
  };
  auto vput = [&](int s) {
#pragma unroll
    for (int i = 0; i < 3; i++) *(LAS u32x4*)(smem + (s & 1) * 12288 + (i * 256 + tid) * 16) = st[i];
  };
  auto half = [&](f32x4 (&acc)[5][2], const bf16x8 (&d)[2][6], auto HF) {
    constexpr int hf = decltype(HF)::value;
#pragma unroll
    for (int ks = 0; ks < 6; ks++)
#pragma unroll
      for (int t = 0; t < 5; t++)
#pragma unroll
        for (int mt = 0; mt < 2; mt++) {
          if (ks == 0) mf0B<true>(acc[t][mt], d[mt][ks], W[5 * hf + t][ks]); else mfaB<true>(acc[t][mt], d[mt][ks], W[5 * hf + t][ks]);
        }
  };
  auto store = [&](f32x4 (&acc)[5][2], int s, int hf) {
#pragma unroll
    for (int t = 0; t < 5; t++) {
      asm volatile("" : "+v"(acc[t][0]), "+v"(acc[t][1]));
      __builtin_amdgcn_raw_buffer_store_b128(pack8(acc[t][0], acc[t][1]), ws, zl + (5 * hf + t) * 2 * 20480, OFF_Z + s * 80 * 20480, 16);
    }
  };
  wait_flags(ws, foff, ftgt);
  TL(8, h == 0);
  vload(0); vput(0);
  __syncthreads();
  for (int s = 0; s < NSEG; s++) {
    const int sn = s + 1 < NSEG ? s + 1 : s;
    const unsigned fo = foff == OOB ? OOB : foff + sn * 512;
    unsigned fl = ld1_sc1(ws, fo);
    bf16x8 cur[2][6];
#pragma unroll
    for (int ks = 0; ks < 6; ks++)
#pragma unroll
      for (int mt = 0; mt < 2; mt++) cur[mt][ks] = *(LAS bf16x8*)(smem + (s & 1) * 12288 + (ks * 2 + mt) * 1024 + lane * 16);
    SB();
    f32x4 acc0[5][2], acc1[5][2];
    GST(1, s, 0, h == 0);
    half(acc0, cur, std::integral_constant<int, 0>());
    SB();
    GST(1, s, 1, h == 0);
    drain(); __syncthreads();
    GST(1, s, 2, h == 0);
    if (s > 3 && tid == 0) atom_add(p.ws, OFF_REC + (s - 1) * 128 + 0, 1);
    const bool vready = __all(fl >= ftgt);
    if (vready) vload(sn);
    store(acc0, s, 0);
    SB();
    GST(1, s, 3, h == 0);
    half(acc1, cur, std::integral_constant<int, 1>());
    asm volatile("s_nop 7" ::: "memory");
    GST(1, s, 4, h == 0);
    store(acc1, s, 1);
    if (s < 3) { drain(); __syncthreads(); if (tid == 0) atom_add(p.ws, OFF_REC + s * 128 + 0, 1); }
    if (!vready) { wait_flags(ws, fo, ftgt); vload(sn); }
    vput(sn + (s + 1 < NSEG ? 0 : 1));
    __syncthreads();
  }
  drain(); __syncthreads();
  if (tid == 0) atom_add(p.ws, OFF_REC + (NSEG - 1) * 128 + 0, 1);
  TL(9, h == 0);

}

DEVI void role_GU(const Params& p, const int wv, int c, LAS char* smem, rsrc_t ws) {
  int tid = wv * 64 + lane_id(); asm volatile("; roleGU" : "+v"(tid) :: "memory");
  const int lane = tid & 63, w = __builtin_amdgcn_readfirstlane(tid >> 6);
  const unsigned lds0 = (unsigned)(size_t)smem;
  bf16x8 W[4][20];
  x_dma(p, 0, lds0 + 81920, w, lane);
  {
    rsrc_t rg = __builtin_amdgcn_make_buffer_rsrc((void*)p.Wg, 0, 640 * 1536 * 2, 0x00020000);
    rsrc_t ru = __builtin_amdgcn_make_buffer_rsrc((void*)p.Wu, 0, 640 * 1536 * 2, 0x00020000);
    u32x4 pre[5][8];
    auto issue = [&](int ch) {
#pragma unroll
      for (int i = 0; i < 8; i++) { int u = i * 256 + tid; int row = (u >> 4) & 63, pc = u & 15;
        pre[ch % 5][i] = __builtin_amdgcn_raw_buffer_load_b128(i < 4 ? rg : ru, row * 3072 + pc * 16, ch * 64 * 3072 + c * 256, 0); }
    };
    issue(0); issue(1); issue(2); issue(3); issue(4);
    const int p4 = lane & 3;
#pragma unroll
    for (int ch = 0; ch < 10; ch++) {
#pragma unroll
      for (int i = 0; i < 8; i++) { const int u = i * 256 + tid; *(LAS u32x4*)(smem + (u >> 4) * 304 + (u & 15) * 16) = pre[ch % 5][i]; }
      __syncthreads();
      if (ch + 5 < 10) issue(ch + 5);
#pragma unroll
      for (int k2 = 0; k2 < 2; k2++)
#pragma unroll
        for (int t = 0; t < 4; t++) W[t][2 * ch + k2] = tr8(smem + (t >> 1) * (64 * 304), 304, 32 * k2, (32 * w + 8 * p4 + 4 * (t & 1)) * 2, lane);
      __syncthreads();
    }
  }
  drain(); __syncthreads();
  for (int s = 0; s < NSEG; s++) {
    LAS char* xb = smem + (((s + 1) & 1) ? 81920 : 0);
    const int sn = s + 1 < NSEG ? s + 1 : s;
    const char* src = p.ws + OFF_XNF + sn * 40960 + w * 10240 + lane * 16;
    const unsigned ldst = lds0 + ((s & 1) ? 81920 : 0) + w * 10240;
    f32x4 acc[4][2];
    dense_phase<4, 20>(acc, W, xb, lane, [&](int kk) { if (kk < 10) glds16(src + kk * 1024, ldst + kk * 1024); });
    drain(); __syncthreads();
    if (s > 0 && tid == 0) atom_add(p.ws, OFF_REC + (s - 1) * 128 + 4, 1);
#pragma unroll
    for (int tt = 0; tt < 2; tt++) {
      f32x4 h0, h1;
#pragma unroll
      for (int r = 0; r < 4; r++) { h0[r] = rbf(acc[0][tt][r]) * rbf(acc[2][tt][r]); h1[r] = rbf(acc[1][tt][r]) * rbf(acc[3][tt][r]); }
      __builtin_amdgcn_raw_buffer_store_b128(pack8(h0, h1), ws, lane * 16, OFF_HF + ((s * 48 + 4 * c + w) * 2 + tt) * 1024, 16);
    }
  }
  drain(); __syncthreads();
  if (tid == 0) atom_add(p.ws, OFF_REC + (NSEG - 1) * 128 + 4, 1);

}

DEVI void role_DN(const Params& p, const int wv, int cc, LAS char* smem, rsrc_t ws) {
  const int c = cc % 5, par = cc / 5;
  int tid = wv * 64 + lane_id(); asm volatile("; roleDN" : "+v"(tid) :: "memory");
  const int lane = tid & 63, w = __builtin_amdgcn_readfirstlane(tid >> 6);
  bf16x8 W[2][48];
  {
    rsrc_t rd = __builtin_amdgcn_make_buffer_rsrc((void*)p.Wd, 0, 1536 * 640 * 2, 0x00020000);
    u32x4 pre[3][8];
    auto issue = [&](int ch) {
#pragma unroll
      for (int i = 0; i < 8; i++) { int u = i * 256 + tid; int row = u >> 4, pc = u & 15;
        pre[ch % 3][i] = __builtin_amdgcn_raw_buffer_load_b128(rd, row * 1280 + pc * 16, ch * 128 * 1280 + c * 256, 0); }
    };
    issue(0); issue(1); issue(2);
    const int p4 = lane & 3;
#pragma unroll
    for (int ch = 0; ch < 12; ch++) {
#pragma unroll
      for (int i = 0; i < 8; i++) { const int u = i * 256 + tid; *(LAS u32x4*)(smem + (u >> 4) * 304 + (u & 15) * 16) = pre[ch % 3][i]; }
      __syncthreads();
      if (ch + 3 < 12) issue(ch + 3);
#pragma unroll
      for (int k4 = 0; k4 < 4; k4++)
#pragma unroll
        for (int t = 0; t < 2; t++) W[t][4 * ch + k4] = tr8(smem, 304, 32 * k4, (32 * w + 8 * p4 + 4 * t) * 2, lane);
      __syncthreads();
    }
  }
  const unsigned foff = lane == 0 ? (unsigned)(OFF_REC + 4) : OOB;
  const unsigned ftgt = lane == 0 ? 12u : 0u;
  const int n = lane & 15, g = lane >> 4;
  u32x4 st[12];
  auto hload = [&](int s, int hf) {
#pragma unroll
    for (int i = 0; i < 12; i++) st[i] = __builtin_amdgcn_raw_buffer_load_b128(ws, (i * 256 + tid) * 16, OFF_HF + s * 98304 + hf * 49152, 16);
  };
  wait_flags(ws, foff == OOB ? OOB : foff + par * 128, ftgt);
  hload(par, 0);
  for (int s = par; s < NSEG; s += 2) {
    f32x4 acc[2][2];
#pragma unroll
    for (int hf = 0; hf < 2; hf++) {
#pragma unroll
      for (int i = 0; i < 12; i++) *(LAS u32x4*)(smem + hf * 49152 + (i * 256 + tid) * 16) = st[i];
      GST(2, s, 3 * hf, cc == 0);
      if (hf == 0) drain();
      __syncthreads();
      GST(2, s, 3 * hf + 1, cc == 0);
      if (hf == 0) { if (s > 1 && tid == 0) atom_add(p.ws, OFF_REC + (s - 2) * 128 + 8, 1); hload(s, 1); }
      else if (s + 2 < NSEG) { wait_flags(ws, foff == OOB ? OOB : foff + (s + 2) * 128, ftgt); hload(s + 2, 0); }
      GST(2, s, 3 * hf + 2, cc == 0);
      LAS char* xb = smem + hf * 49152;
      bf16x8 b[3][2];
#pragma unroll
      for (int q = 0; q < 2; q++) { b[q][0] = *(LAS bf16x8*)(xb + (q * 2) * 1024 + lane * 16); b[q][1] = *(LAS bf16x8*)(xb + (q * 2 + 1) * 1024 + lane * 16); }
#pragma unroll
      for (int kk = 0; kk < 24; kk++) {
        if (kk + 2 < 24) { b[(kk + 2) % 3][0] = *(LAS bf16x8*)(xb + ((kk + 2) * 2) * 1024 + lane * 16); b[(kk + 2) % 3][1] = *(LAS bf16x8*)(xb + ((kk + 2) * 2 + 1) * 1024 + lane * 16); }
        SB();
        const int kg = hf * 24 + kk;
#pragma unroll
        for (int t = 0; t < 2; t++)
#pragma unroll
          for (int tt = 0; tt < 2; tt++) {
            if (kg == 0) mf0<true>(acc[t][tt], W[t][kg], b[kk % 3][tt]);
            else if (t * 48 + kg < 64) mfa<true>(acc[t][tt], W[t][kg], b[kk % 3][tt]);
            else mfa<false>(acc[t][tt], W[t][kg], b[kk % 3][tt]);
          }
        SB();
      }
    }
    asm volatile("s_nop 7" ::: "memory");
#pragma unroll
    for (int tt = 0; tt < 2; tt++) {
      asm volatile("" : "+v"(acc[0][tt]), "+v"(acc[1][tt]));
      f32x4 g0, g1;
#pragma unroll
      for (int r = 0; r < 4; r++) {
        float d0 = rbf(acc[0][tt][r]), d1 = rbf(acc[1][tt][r]);
        g0[r] = d0 / (1.0f + __expf(-d0)); g1[r] = d1 / (1.0f + __expf(-d1));
      }
      const int tok = 32 * s + 8 * (n >> 2) + 4 * tt + (n & 3);
      __builtin_amdgcn_raw_buffer_store_b128(pack8(g0, g1), ws, OFF_G + tok * 1280 + (128 * c + 32 * w + 8 * g) * 2, 0, 16);
    }
  }
  drain(); __syncthreads();
  if (tid == 0) atom_add(p.ws, OFF_REC + (NSEG - 2 + par) * 128 + 8, 1);
  TL(10, cc == 0);

}

DEVI void token_A(const Params& p, int s, int h, rsrc_t ws, int lane) {
  const int n = lane & 15, g = lane >> 4;
  wait_flags_slow(ws, lane == 0 ? (unsigned)((s * 128 + h) * 4) : lane == 1 ? (unsigned)((s * 128 + 32 + h) * 4) : OOB, lane < 2 ? 6u : 0u);
  bf16x8 qf[2][6], kf[2][6], km[6];
#pragma unroll
  for (int ks = 0; ks < 6; ks++)
#pragma unroll
    for (int tt = 0; tt < 2; tt++) {
      qf[tt][ks] = __builtin_bit_cast(bf16x8, ldx(ws, lane * 16, OFF_QF + ((s * 192 + h * 6 + ks) * 2 + tt) * 1024));
      kf[tt][ks] = __builtin_bit_cast(bf16x8, ldx(ws, lane * 16, OFF_KF + ((s * 192 + h * 6 + ks) * 2 + tt) * 1024));
    }
  SB();
  wait_flags_slow(ws, lane == 0 ? (unsigned)(OFF_KMF + (s * 32 + h) * 128) : OOB, lane == 0 ? 1u : 0u, s >= NSEG - 1);
#pragma unroll
  for (int ks = 0; ks < 6; ks++) km[ks] = __builtin_bit_cast(bf16x8, ldx(ws, g * 128 + (n & 7) * 16, OFF_KM + ((s * 32 + h) * 6 + ks) * 512));
  const float cs = 0.07216878364870322f * 1.4426950408889634f;
#pragma unroll
  for (int tq = 0; tq < 2; tq++) {
    f32x4 T0 = {0, 0, 0, 0}, T1 = {0, 0, 0, 0}, T2 = {0, 0, 0, 0};
#pragma unroll
    for (int ks = 0; ks < 6; ks++) {
      T0 = __builtin_amdgcn_mfma_f32_16x16x32_bf16(km[ks], qf[tq][ks], T0, 0, 0, 0);
      T1 = __builtin_amdgcn_mfma_f32_16x16x32_bf16(kf[0][ks], qf[tq][ks], T1, 0, 0, 0);
      T2 = __builtin_amdgcn_mfma_f32_16x16x32_bf16(kf[1][ks], qf[tq][ks], T2, 0, 0, 0);
    }
    const int qt = 8 * (n >> 2) + 4 * tq + (n & 3);
    float m = fmaxf(fmaxf(T0[0], T0[1]), fmaxf(T0[2], T0[3]));
#pragma unroll
    for (int r = 0; r < 4; r++) {
      if (8 * g + r > qt) T1[r] = -3.0e38f;
      if (8 * g + 4 + r > qt) T2[r] = -3.0e38f;
      m = fmaxf(m, fmaxf(T1[r], T2[r]));
    }
    m = xor32max(xor16max(m));
    const float mc = m * cs;
    float l0 = 0, lx = 0; f32x4 E0, E1, E2;
#pragma unroll
    for (int r = 0; r < 4; r++) {
      E0[r] = __builtin_amdgcn_exp2f(__builtin_fmaf(T0[r], cs, -mc));
      E1[r] = (8 * g + r > qt) ? 0.f : __builtin_amdgcn_exp2f(__builtin_fmaf(T1[r], cs, -mc));
      E2[r] = (8 * g + 4 + r > qt) ? 0.f : __builtin_amdgcn_exp2f(__builtin_fmaf(T2[r], cs, -mc));
      l0 += E0[r]; lx += E1[r] + E2[r];
    }
    const float inv = 1.0f / (xor16add(l0) + xor32add(xor16add(lx)));
    E0 *= inv; E1 *= inv; E2 *= inv;
    const unsigned so = OFF_PX + (s * 160 + h * 5) * 512 + tq * 256;
    __builtin_amdgcn_raw_buffer_store_b64((u32x2){pk2(E0[0], E0[1]), pk2(E0[2], E0[3])}, ws, g < 2 ? (unsigned)(n * 16 + g * 8) : OOB, so, 16);
    __builtin_amdgcn_raw_buffer_store_b128(pack8(E1, E2), ws, (1 + g) * 512 + n * 16, so, 16);
  }
  drain();
  if (lane == 0) atom_add(p.ws, OFF_REC + s * 128 + 16, 1);
}

DEVI void token_B(const Params& p, int s, int cg, LAS char* smem, rsrc_t ws, int tid, int lane, int w) {
  const int n = lane & 15, g = lane >> 4;
  const unsigned zb = (16 * cg + 4 * w + (n >> 3)) * 20480 + (n & 7) * 16;
  const unsigned zx = zb + (1 + g) * 128, zy = zb + g * 640;
  const unsigned zs = OFF_Z + s * 80 * 20480;
  if (w == 0) wait_flags_slow(ws, lane < 2 ? (unsigned)(OFF_REC + s * 128 + (lane == 0 ? 0 : 8)) : OOB, lane == 0 ? 32u : lane == 1 ? 5u : 0u);
  __syncthreads();
  bf16x8 z[40][2];
#pragma unroll
  for (int q = 0; q < 28; q++)
#pragma unroll
    for (int u = 0; u < 2; u++) z[q][u] = __builtin_bit_cast(bf16x8, ldx(ws, zx + u * 2 * 20480 + q * 640, zs));
  u32x2 xv[2][2], gv[2][2];
#pragma unroll
  for (int u = 0; u < 2; u++)
#pragma unroll
    for (int tq = 0; tq < 2; tq++) {
      const int tok = 32 * s + 8 * (n >> 2) + 4 * tq + (n & 3);
      const int col = 128 * cg + 32 * w + 16 * u + 4 * g;
      xv[u][tq] = *(const u32x2*)(p.x + (size_t)tok * 640 + col);
      gv[u][tq] = __builtin_amdgcn_raw_buffer_load_b64(ws, OFF_G + tok * 1280 + col * 2, 0, 16);
    }
  SB();
  if (w == 0) {
    wait_flags_slow(ws, OFF_YF + s * 512 + lane * 4, 1u, s >= NSEG - 2);
    wait_flags_slow(ws, lane < 16 ? (unsigned)(OFF_YF + s * 512 + 256 + lane * 4) : OOB, lane < 16 ? 1u : 0u, s >= NSEG - 2);
    wait_flags_slow(ws, lane == 0 ? (unsigned)(OFF_REC + s * 128 + 16) : OOB, lane == 0 ? 32u : 0u, s >= NSEG - 2);
  }
  __syncthreads();
  u32x4 st[20];
#pragma unroll
  for (int i = 0; i < 20; i++) st[i] = ldx(ws, (i * 256 + tid) * 16, OFF_PX + s * 81920);
#pragma unroll
  for (int q = 28; q < 32; q++)
#pragma unroll
    for (int u = 0; u < 2; u++) z[q][u] = __builtin_bit_cast(bf16x8, ldx(ws, zx + u * 2 * 20480 + q * 640, zs));
#pragma unroll
  for (int q = 32; q < 40; q++)
#pragma unroll
    for (int u = 0; u < 2; u++) z[q][u] = __builtin_bit_cast(bf16x8, ldx(ws, zy + u * 2 * 20480 + (q - 32) * 2560, zs));
  SB();
#pragma unroll
  for (int i = 0; i < 20; i++) *(LAS u32x4*)(smem + (i * 256 + tid) * 16) = st[i];
  __syncthreads();
  f32x4 acc[2][2] = {{{0, 0, 0, 0}, {0, 0, 0, 0}}, {{0, 0, 0, 0}, {0, 0, 0, 0}}};
#pragma unroll
  for (int ks = 0; ks < 40; ks++) {
    const int cb = ks < 32 ? (5 * ks + 1) * 512 + g * 512 : (20 * (ks - 32)) * 512 + g * 2560;
    bf16x8 b0 = *(LAS bf16x8*)(smem + cb + n * 16), b1 = *(LAS bf16x8*)(smem + cb + 256 + n * 16);
    acc[0][0] = __builtin_amdgcn_mfma_f32_16x16x32_bf16(z[ks][0], b0, acc[0][0], 0, 0, 0);
    acc[0][1] = __builtin_amdgcn_mfma_f32_16x16x32_bf16(z[ks][0], b1, acc[0][1], 0, 0, 0);
    acc[1][0] = __builtin_amdgcn_mfma_f32_16x16x32_bf16(z[ks][1], b0, acc[1][0], 0, 0, 0);
    acc[1][1] = __builtin_amdgcn_mfma_f32_16x16x32_bf16(z[ks][1], b1, acc[1][1], 0, 0, 0);
  }
#pragma unroll
  for (int u = 0; u < 2; u++)
#pragma unroll
    for (int tq = 0; tq < 2; tq++) {
      const int tok = 32 * s + 8 * (n >> 2) + 4 * tq + (n & 3);
      const int col = 128 * cg + 32 * w + 16 * u + 4 * g;
      float o[4];
#pragma unroll
      for (int r = 0; r < 4; r++) {
        unsigned xw = r < 2 ? xv[u][tq].x : xv[u][tq].y, gw = r < 2 ? gv[u][tq].x : gv[u][tq].y;
        float xf = __uint_as_float((r & 1) ? (xw & 0xffff0000u) : (xw << 16)), gf = __uint_as_float((r & 1) ? (gw & 0xffff0000u) : (gw << 16));
        o[r] = xf + rbf(rbf(acc[u][tq][r]) * gf);
      }
      *(u32x2*)(p.out + (size_t)tok * 640 + col) = (u32x2){pk2(o[0], o[1]), pk2(o[2], o[3])};
    }
  __syncthreads();
}

DEVI void role_TK(const Params& p, const int wv, int t, LAS char* smem, rsrc_t ws) {
  int tid = wv * 64 + lane_id(); asm volatile("; roleTK" : "+v"(tid) :: "memory");
  const int lane = tid & 63, w = wv;
  LAS unsigned* qw = (LAS unsigned*)(smem + 98304);
  __syncthreads();
  if (tid == 0) *qw = __hip_atomic_fetch_add((unsigned*)(p.ws + OFF_TQ), 1u, __ATOMIC_RELAXED, __HIP_MEMORY_SCOPE_AGENT);
  __syncthreads();
  int idx = __builtin_amdgcn_readfirstlane((int)*qw);
  while (idx < (NSEG + 2) * 13) {
    const int sl = idx / 13, k = idx - sl * 13;
    if (k < 8) { if (sl < NSEG) token_A(p, sl, 4 * k + w, ws, lane); }
    else if (sl >= 2) token_B(p, sl - 2, k - 8, smem, ws, tid, lane, w);
    __syncthreads();
    if (tid == 0) *qw = __hip_atomic_fetch_add((unsigned*)(p.ws + OFF_TQ), 1u, __ATOMIC_RELAXED, __HIP_MEMORY_SCOPE_AGENT);
    __syncthreads();
    idx = __builtin_amdgcn_readfirstlane((int)*qw);
  }
  TL(16 + (t & 31), true);
  __builtin_amdgcn_endpgm(); __builtin_unreachable();
}

constexpr int PB = 256;
DEVI void ln_row(const Params& p, int tok, u32x4 v0, u32x4 v1, int lane) {
  float f[16];
#pragma unroll
  for (int i = 0; i < 4; i++) {
    unsigned a = v0[i], c = v1[i];
    f[2 * i] = __uint_as_float(a << 16); f[2 * i + 1] = __uint_as_float(a & 0xffff0000u);
    f[8 + 2 * i] = __uint_as_float(c << 16); f[8 + 2 * i + 1] = __uint_as_float(c & 0xffff0000u);
  }
  float sum = 0;
#pragma unroll
  for (int i = 0; i < 16; i++) sum += f[i];
  for (int o = 32; o; o >>= 1) sum += __shfl_xor(sum, o);
  const float mean = sum * (1.0f / 640.0f);
  float sq = 0;
#pragma unroll
  for (int i = 0; i < 8; i++) { float d = f[i] - mean; sq += d * d; }
  if (lane < 16) {
#pragma unroll
    for (int i = 8; i < 16; i++) { float d = f[i] - mean; sq += d * d; }
  }
  for (int o = 32; o; o >>= 1) sq += __shfl_xor(sq, o);
  const float mb = rbf(mean), vb = rbf(sq * (1.0f / 640.0f));
  const float den = rbf(sqrtf(rbf(vb + 1e-5f)));
  float y[16];
#pragma unroll
  for (int i = 0; i < 16; i++) y[i] = rbf(f[i] - mb) / den;
  const int s = tok >> 5, t = tok & 31, tt = (t >> 2) & 1, n = 4 * (t >> 3) + (t & 3);
  int pc = lane; int cb = pc >> 2, g = pc & 3;
  *(u32x4*)(p.ws + OFF_XNF + ((s * 20 + cb) * 2 + tt) * 1024 + (n + 16 * g) * 16) = (u32x4){pk2(y[0], y[1]), pk2(y[2], y[3]), pk2(y[4], y[5]), pk2(y[6], y[7])};
  if (lane < 16) { pc = 64 + lane; cb = pc >> 2; g = pc & 3;
    *(u32x4*)(p.ws + OFF_XNF + ((s * 20 + cb) * 2 + tt) * 1024 + (n + 16 * g) * 16) = (u32x4){pk2(y[8], y[9]), pk2(y[10], y[11]), pk2(y[12], y[13]), pk2(y[14], y[15])}; }
}
__global__ __launch_bounds__(256) void k_init(Params p) {
  const int tid = threadIdx.x, lane = tid & 63, w = tid >> 6;
  const int b = blockIdx.x; const int wv = w;
  TL(11, b == 0);
  u32x4 xv[2][2];
#pragma unroll
  for (int r = 0; r < 2; r++) {
    const uint16_t* xr = p.x + (size_t)(b * 8 + w * 2 + r) * 640;
    xv[r][0] = *(const u32x4*)(xr + lane * 8); xv[r][1] = (u32x4){0, 0, 0, 0};
    if (lane < 16) xv[r][1] = *(const u32x4*)(xr + 512 + lane * 8);
  }
  const u32x4 ones = {SENT, SENT, SENT, SENT}, zero = {0, 0, 0, 0};
  for (unsigned o = OFF_MEMB + 10240 + (b * 256 + tid) * 16; o < SENT_END; o += PB * 256 * 16) *(u32x4*)(p.ws + o) = ones;
  for (unsigned o = (b * 256 + tid) * 16; o < FLAGS_END; o += PB * 256 * 16) *(u32x4*)(p.ws + o) = zero;
  if (b == 255) {
    for (int u = tid; u < 640; u += 256) {
      int j = u >> 3, i = u & 7;
      u32x4 v = *(const u32x4*)(p.mem0 + i * 640 + j * 8);
      v.x = san(v.x); v.y = san(v.y); v.z = san(v.z); v.w = san(v.w);
      *(u32x4*)(p.ws + OFF_MEMB + j * 128 + i * 16) = v;
    }
  }
#pragma unroll
  for (int r = 0; r < 2; r++) ln_row(p, b * 8 + w * 2 + r, xv[r][0], xv[r][1], lane);
  TL(13, b == 0);
}

DEVI void b_task(const Params& p, const int wv, int h, int nb, LAS char* sm, rsrc_t ws) {
  int tid = wv * 64 + lane_id(); asm volatile("; btask" : "+v"(tid) :: "memory");
  const int lane = tid & 63, w = wv;
  const int m = lane & 15, g = lane >> 4;
  u32x4 t15[15];
#pragma unroll
  for (int i = 0; i < 15; i++) { int u = i * 256 + tid; int row = u / 20, pc = u - row * 20; t15[i] = *(const u32x4*)(p.Wo + ((size_t)h * 192 + row) * 640 + 160 * nb + pc * 8); }
  bf16x8 an[4][6];
  rsrc_t rv = __builtin_amdgcn_make_buffer_rsrc((void*)p.Wv, 0, 640 * 6144 * 2, 0x00020000);
  const unsigned avo = m * 12288 + h * 384 + g * 16;
#pragma unroll
  for (int mt = 0; mt < 4; mt++)
#pragma unroll
    for (int ks = 0; ks < 6; ks++) an[mt][ks] = __builtin_bit_cast(bf16x8, __builtin_amdgcn_raw_buffer_load_b128(rv, avo + mt * (16 * 12288) + ks * 64, w * 64 * 12288, 0));
#pragma unroll
  for (int i = 0; i < 15; i++) { int u = i * 256 + tid; int row = u / 20, pc = u - row * 20; *(LAS u32x4*)(sm + row * 336 + pc * 16) = t15[i]; }
  __syncthreads();
  TL(60, h == 0 && nb == 0);
  bf16x8 Bf[10][6];
  const int p4 = lane & 3;
#pragma unroll
  for (int nt = 0; nt < 10; nt++)
#pragma unroll
    for (int ks = 0; ks < 6; ks++) Bf[nt][ks] = tr8(sm, 336, 32 * ks, (16 * nt + 4 * p4) * 2, lane);
  const unsigned bo = (((160 * nb + m) >> 3) * 16 + (h >> 1)) * 20480 + ((h & 1) * 8 + (m & 7) + 16 * g) * 16;
#pragma unroll 1
  for (int kk = w; kk < 10; kk += 4) {
    const int kn = kk + 4 < 10 ? kk + 4 : kk;
#pragma unroll
    for (int hf = 0; hf < 4; hf++) {
      const int u = hf & 1, c5 = (hf >> 1) * 5;
      f32x4 acc[2][5];
#pragma unroll
      for (int ks = 0; ks < 6; ks++)
#pragma unroll
        for (int a = 0; a < 2; a++)
#pragma unroll
          for (int nt = 0; nt < 5; nt++) acc[a][nt] = __builtin_amdgcn_mfma_f32_16x16x32_bf16(an[u + 2 * a][ks], Bf[c5 + nt][ks], ks == 0 ? (f32x4){0, 0, 0, 0} : acc[a][nt], 0, 0, 0);
#pragma unroll
      for (int nt = 0; nt < 5; nt++) {
        const f32x4 &A = acc[0][nt], &B = acc[1][nt];
        u32x4 v = {pk2(A[0], A[1]), pk2(B[0], B[1]), pk2(A[2], A[3]), pk2(B[2], B[3])};
        __builtin_amdgcn_raw_buffer_store_b128(v, ws, bo + (c5 + nt) * (2 * 16 * 20480) + u * 1024, OFF_BF + kk * 2048, 16);
      }
      if (hf >= 2) {
#pragma unroll
        for (int a = 0; a < 2; a++)
#pragma unroll
          for (int ks = 0; ks < 6; ks++) an[u + 2 * a][ks] = __builtin_bit_cast(bf16x8, __builtin_amdgcn_raw_buffer_load_b128(rv, avo + (u + 2 * a) * (16 * 12288) + ks * 64, kn * 64 * 12288, 0));
      }
      SB();
    }
  }
  TL(62, h == 0 && nb == 0);
  drain(); __syncthreads();
  if (tid == 0) atom_add(p.ws, OFF_BRDY + nb * 128, 1);
}

__global__ __launch_bounds__(256, 1) void k_main(Params p) {
  extern __shared__ __attribute__((aligned(16))) char smem_[];
  LAS char* smem = (LAS char*)smem_;
  rsrc_t ws = __builtin_amdgcn_make_buffer_rsrc(p.ws, 0, 0x7fffffff, 0x00020000);
  int b = blockIdx.x;
  const int wv = __builtin_amdgcn_readfirstlane(threadIdx.x >> 6);
  TL(0, b == 0);
  {
    constexpr int Q0 = N_S + N_V, NQ = 19, G0 = N_S + N_V + N_QKV + N_VO;
    const int x = b & 7;
    const int nq = (NQ - ((x - Q0) & 7) + 7) >> 3, ng = (256 - G0 - ((x - G0) & 7) + 7) >> 3;
    int rank = -1;
    if (b >= Q0 && b < Q0 + NQ) rank = (b - Q0) >> 3;
    else if (b >= G0) rank = nq + ((b - G0) >> 3);
    else if (b >= N_S && b < N_S + N_V) rank = nq + ng + ((b - N_S) >> 3);
    if (rank >= 0 && rank < 16 && (p.mask & 128)) b_task(p, wv, x + 8 * (rank & 3), rank >> 2, smem, ws);
  }
  TL(12, b == 0); TL(14, b == 40); TL(15, b == 120);
  if (b < N_S) { if (p.mask & 1) role_S(p, wv, b, smem, ws); return; }
  b -= N_S;
  if (b < N_V) { if (p.mask & 2) role_V(p, wv, b, smem, ws); return; }
  b -= N_V;
  if (b < N_QKV) { if (p.mask & 4) role_QKV(p, wv, b, smem, ws); }
  else if (b < N_QKV + N_VO) { if (p.mask & 8) role_VO(p, wv, b - N_QKV, smem, ws); }
  else if (b < N_QKV + N_VO + N_GU) { if (p.mask & 16) role_GU(p, wv, b - N_QKV - N_VO, smem, ws); }
  else if (b < N_QKV + N_VO + N_GU + N_DN) { if (p.mask & 32) role_DN(p, wv, b - N_QKV - N_VO - N_GU, smem, ws); }
  if (p.mask & 64) role_TK(p, wv, b, smem, ws);
}

static void launch_all(void* const* d_in, void* d_out, void* d_ws, const int* tune, const unsigned* masks, int nmasks, int do_pre, hipStream_t stream) {
  Params p;
  p.x = (const uint16_t*)d_in[0]; p.mem0 = (const uint16_t*)d_in[1];
  p.Wq = (const uint16_t*)d_in[2]; p.Wk = (const uint16_t*)d_in[3]; p.Wv = (const uint16_t*)d_in[4]; p.Wo = (const uint16_t*)d_in[5];
  p.Wg = (const uint16_t*)d_in[8]; p.Wu = (const uint16_t*)d_in[9]; p.Wd = (const uint16_t*)d_in[10];
  p.out = (uint16_t*)d_out; p.ws = (char*)d_ws;
  p.nsteps = NSEG; p.dS = tune[0]; p.dVm = tune[1]; p.dVe = tune[2]; p.dVl = tune[3]; p.inc = tune[4]; p.dec = tune[5]; p.mask = 0;
  (void)hipFuncSetAttribute((const void*)k_main, hipFuncAttributeMaxDynamicSharedMemorySize, 160 * 1024);
  if (do_pre) k_init<<<PB, 256, 0, stream>>>(p);
  for (int i = 0; i < nmasks; i++) { p.mask = masks[i]; k_main<<<256, 256, 160 * 1024, stream>>>(p); }
}
extern "C" void debug_launch(void* const* d_in, void* d_out, void* d_ws, const int* tune, const unsigned* masks, int nmasks, int do_pre, hipStream_t stream) {
  launch_all(d_in, d_out, d_ws, tune, masks, nmasks, do_pre, stream);
}
extern "C" void kernel_launch(void* const* d_in, const int* in_sizes, int n_in, void* d_out, int out_size, void* d_ws, size_t ws_size, hipStream_t stream) {
  const int tune[6] = {202, 202, 90, 119, 24, 2};
  const unsigned masks[1] = {255};
  launch_all(d_in, d_out, d_ws, tune, masks, 1, 1, stream);
}
```

```cpp
#include <hip/hip_runtime.h>
#include <stdint.h>
#include <type_traits>
typedef __attribute__((ext_vector_type(8), may_alias)) __bf16 bf16x8;
typedef __attribute__((ext_vector_type(16), may_alias)) __bf16 bf16x16;
typedef __attribute__((ext_vector_type(4), may_alias)) __bf16 bf16x4;
typedef __attribute__((ext_vector_type(2), may_alias)) __bf16 bf16x2;
typedef __attribute__((ext_vector_type(4), may_alias)) float f32x4;
typedef __attribute__((ext_vector_type(2), may_alias)) float f32x2;
typedef __attribute__((ext_vector_type(4), may_alias)) unsigned u32x4;
typedef __attribute__((ext_vector_type(2), may_alias)) unsigned u32x2;
typedef __amdgpu_buffer_rsrc_t rsrc_t;
#define LAS __attribute__((address_space(3)))
#define SENT 0xFFFFFFFFu
#define DEVI __device__ __forceinline__

constexpr int NSEG = 64;
constexpr unsigned OFF_MEMB = 1u << 20;
constexpr unsigned OFF_EBUF = OFF_MEMB + 768 * 1024;
constexpr unsigned OFF_PMB = OFF_EBUF + (1u << 20);
constexpr unsigned OFF_ALB = OFF_PMB + 256 * 1024;
constexpr unsigned OFF_TB = OFF_ALB + 64 * 1024;
constexpr unsigned SENT_END = OFF_TB + 32 * 1024;
constexpr unsigned OFF_XNF = 4u << 20;
constexpr unsigned OFF_QF = 8u << 20;
constexpr unsigned SZ_QF = 64u * 192 * 2 * 1024;
constexpr unsigned OFF_KF = OFF_QF + SZ_QF;
constexpr unsigned OFF_VF = OFF_QF + 2 * SZ_QF;
constexpr unsigned OFF_KM = OFF_QF + 3 * SZ_QF;
constexpr unsigned OFF_BF = OFF_KM + 64 * 32 * 6 * 512;
constexpr unsigned OFF_Z = OFF_BF + 80 * 16 * 10 * 2048;
constexpr unsigned OFF_PX = OFF_Z + 64u * 80 * 32 * 5 * 128;
constexpr unsigned OFF_HF = OFF_PX + 64 * 160 * 512;
constexpr unsigned OFF_G = OFF_HF + 64 * 96 * 1024;
constexpr unsigned OFF_YB = OFF_G + 2048 * 640 * 2;
constexpr unsigned WS_END = OFF_YB + 64u * 80 * 32 * 128;
static_assert(WS_END <= (256u << 20), "workspace");
constexpr unsigned OFF_REC = 32768;
constexpr unsigned OFF_CH = 40960;
constexpr unsigned OFF_CG = 98304;
constexpr unsigned OFF_CPX = 106496;
constexpr unsigned OFF_BRDY = 49152;
constexpr unsigned OFF_TQ = 57344;
constexpr unsigned OFF_YF = 65536;
constexpr unsigned OFF_KMF = 131072;
constexpr unsigned FLAGS_END = OFF_KMF + 64 * 32 * 128;
constexpr int N_QKV = 58, N_VO = 32, N_GU = 12, N_DN = 10;

struct Params {
  const uint16_t* x; const uint16_t* mem0;
  const uint16_t* Wq; const uint16_t* Wk; const uint16_t* Wv; const uint16_t* Wo;
  const uint16_t* Wg; const uint16_t* Wu; const uint16_t* Wd;
  uint16_t* out;
  char* ws;
};
constexpr int T_MEM = 192, T_E = 86, T_L = 114, T_INC = 16, T_DEC = 2;

DEVI int lane_id() { int l; asm volatile("v_mbcnt_lo_u32_b32 %0, -1, 0\n\tv_mbcnt_hi_u32_b32 %0, -1, %0" : "=v"(l)); return l; }
DEVI u32x4 ld_sc1(rsrc_t r, unsigned off) { return __builtin_amdgcn_raw_buffer_load_b128(r, off, 0, 16); }
DEVI unsigned ld1_sc1(rsrc_t r, unsigned off) { return __builtin_amdgcn_raw_buffer_load_b32(r, off, 0, 16); }
DEVI unsigned mx4(u32x4 v) { return max(max(v.x, v.y), max(v.z, v.w)); }
DEVI unsigned pk2(float a, float b) { bf16x2 v = {(__bf16)a, (__bf16)b}; return __builtin_bit_cast(unsigned, v); }
DEVI unsigned san(unsigned v) { return min(v, 0xFFFFFFFEu); }
DEVI bf16x4 trread(LAS char* p) { return __builtin_amdgcn_ds_read_tr16_b64_v4bf16((LAS bf16x4*)p); }
DEVI float fmx(float a, float b) { float r; asm("v_max_f32 %0, %1, %2" : "=v"(r) : "v"(a), "v"(b)); return r; }
DEVI float fmx3(float a, float b, float c) { float r; asm("v_max3_f32 %0, %1, %2, %3" : "=v"(r) : "v"(a), "v"(b), "v"(c)); return r; }
template <bool AG> DEVI void smf(f32x4& acc, bf16x8 a, const bf16x16& b, int idx) {
  if constexpr (AG) asm volatile("v_smfmac_f32_16x16x64_bf16 %0, %1, %2, %3" : "+v"(acc) : "v"(a), "a"(b), "v"(idx));
  else asm volatile("v_smfmac_f32_16x16x64_bf16 %0, %1, %2, %3" : "+v"(acc) : "v"(a), "v"(b), "v"(idx));
}
DEVI float ror8add(float x) { return x + __uint_as_float(__builtin_amdgcn_update_dpp(0u, __float_as_uint(x), 0x128, 0xf, 0xf, false)); }
DEVI float xor16max(float x) { auto r = __builtin_amdgcn_permlane16_swap(__float_as_uint(x), __float_as_uint(x), false, false); unsigned a = r[0], b = r[1]; return fmx(__uint_as_float(a), __uint_as_float(b)); }
DEVI float xor32max(float x) { auto r = __builtin_amdgcn_permlane32_swap(__float_as_uint(x), __float_as_uint(x), false, false); unsigned a = r[0], b = r[1]; return fmx(__uint_as_float(a), __uint_as_float(b)); }
DEVI float xor16add(float x) { auto r = __builtin_amdgcn_permlane16_swap(__float_as_uint(x), __float_as_uint(x), false, false); unsigned a = r[0], b = r[1]; return __uint_as_float(a) + __uint_as_float(b); }
DEVI float xor32add(float x) { auto r = __builtin_amdgcn_permlane32_swap(__float_as_uint(x), __float_as_uint(x), false, false); unsigned a = r[0], b = r[1]; return __uint_as_float(a) + __uint_as_float(b); }
DEVI f32x4 halfsum2(f32x4 A, f32x4 B) {
  f32x4 o;
#pragma unroll
  for (int r = 0; r < 4; r++) {
    float a = A[r], b = B[r];
    auto s = __builtin_amdgcn_permlane32_swap(__float_as_uint(a), __float_as_uint(b), false, false);
    unsigned s0 = s[0], s1 = s[1];
    o[r] = __uint_as_float(s0) + __uint_as_float(s1);
  }
  return o;
}
constexpr unsigned SPIN_CAP = 1u << 18;


DEVI unsigned rt32() { unsigned long long t; asm volatile("s_memrealtime %0\n\ts_waitcnt lgkmcnt(0)" : "=s"(t) :: "memory"); return (unsigned)t; }
DEVI void wait_rt(unsigned t) { while (true) { const int rem = (int)(t - rt32()); if (rem <= 0 || rem > 4000) break; } }
DEVI int wave_max32(int v) {
  const int lo = (int)0x80000000;
  v = max(v, __builtin_amdgcn_update_dpp(lo, v, 0x111, 0xf, 0xf, false));
  v = max(v, __builtin_amdgcn_update_dpp(lo, v, 0x112, 0xf, 0xf, false));
  v = max(v, __builtin_amdgcn_update_dpp(lo, v, 0x114, 0xf, 0xf, false));
  v = max(v, __builtin_amdgcn_update_dpp(lo, v, 0x118, 0xf, 0xf, false));
  v = max(v, __builtin_amdgcn_update_dpp(lo, v, 0x142, 0xa, 0xf, false));
  return __builtin_amdgcn_readlane(v, 31);
}
DEVI unsigned beacon_max(rsrc_t ws, int s, int lane, unsigned ref) {
  unsigned b = __builtin_amdgcn_raw_buffer_load_b32(ws, (lane & 31) * 16, OFF_TB + s * 512, 16);
  if (__builtin_amdgcn_ballot_w64(b == SENT) != 0) {
    for (unsigned spins = 0; spins < SPIN_CAP; spins++) {
      asm volatile("" ::: "memory");
      b = __builtin_amdgcn_raw_buffer_load_b32(ws, (lane & 31) * 16, OFF_TB + s * 512, 16);
      if (__builtin_amdgcn_ballot_w64(b == SENT) == 0) break;
    }
  }
  return ref + (unsigned)wave_max32((int)(b - ref));
}
DEVI void adapt(int& D, bool fail, int floor4) { D = fail ? D + T_INC : max(D - T_DEC, floor4); }

DEVI u32x4 ldx(rsrc_t r, unsigned voff, unsigned soff) { return __builtin_amdgcn_raw_buffer_load_b128(r, voff, soff, 16); }
DEVI bool has_sent(unsigned m) { return __builtin_amdgcn_ballot_w64(m == SENT) != 0; }
#define SB() __builtin_amdgcn_sched_barrier(0)
DEVI bool gather_mem(rsrc_t ws, int s, LAS char* mlw, int w, unsigned voff) {
  const unsigned so = OFF_MEMB + s * 10240;
  bool fail = false;
  if (w < 2) {
    u32x4 v0 = ldx(ws, voff, so), v1 = ldx(ws, voff + 4096, so), v2 = ldx(ws, voff + 8192, so);
    SB();
    if (has_sent(max(max(mx4(v0), mx4(v1)), mx4(v2)))) {
      fail = true;
      for (unsigned spins = 0; spins < SPIN_CAP; spins++) {
        asm volatile("" ::: "memory");
        v0 = ldx(ws, voff, so); v1 = ldx(ws, voff + 4096, so); v2 = ldx(ws, voff + 8192, so);
        SB();
        if (!has_sent(max(max(mx4(v0), mx4(v1)), mx4(v2)))) break;
      }
    }
    *(LAS u32x4*)(mlw) = v0; *(LAS u32x4*)(mlw + 4096) = v1; *(LAS u32x4*)(mlw + 8192) = v2;
  } else {
    u32x4 v0 = ldx(ws, voff, so), v1 = ldx(ws, voff + 4096, so);
    SB();
    if (has_sent(max(mx4(v0), mx4(v1)))) {
      fail = true;
      for (unsigned spins = 0; spins < SPIN_CAP; spins++) {
        asm volatile("" ::: "memory");
        v0 = ldx(ws, voff, so); v1 = ldx(ws, voff + 4096, so);
        SB();
        if (!has_sent(max(mx4(v0), mx4(v1)))) break;
      }
    }
    *(LAS u32x4*)(mlw) = v0; *(LAS u32x4*)(mlw + 4096) = v1;
  }
  return fail;
}

DEVI bf16x16 cat16(bf16x8 lo, bf16x8 hi) { return __builtin_shufflevector(lo, hi, 0, 1, 2, 3, 4, 5, 6, 7, 8, 9, 10, 11, 12, 13, 14, 15); }

DEVI bf16x16 tr_frag(LAS char* tile, int ldb, int c0, int lane) {
  const int bg = lane >> 4, q4 = (lane & 15) >> 2, p4 = lane & 3;
  bf16x4 e4[4];
#pragma unroll
  for (int jj = 0; jj < 4; jj++) {
    int g = 2 * (jj >> 1) + (bg >> 1), pp = 2 * (bg & 1) + (jj & 1);
    int row = 8 * g + 2 * pp + (q4 & 1) + 32 * (q4 >> 1);
    e4[jj] = trread(tile + row * ldb + (row >> 5) * 32 + (c0 + 4 * p4) * 2);
  }
  bf16x16 f;
#pragma unroll
  for (int jj = 0; jj < 4; jj++)
#pragma unroll
    for (int e = 0; e < 4; e++) f[4 * jj + e] = e4[jj][e];
  return f;
}

struct AFr { bf16x8 a[10]; };
DEVI void a_load(AFr& A, LAS char* ml, int aoff) {
#pragma unroll
  for (int q = 0; q < 10; q++) A.a[q] = *(LAS bf16x8*)(ml + q * 1024 + aoff);
}
DEVI bf16x16 fb_load(LAS char* wl, int kk, int lane) { return cat16(*(LAS bf16x8*)(wl + kk * 2048 + lane * 16), *(LAS bf16x8*)(wl + kk * 2048 + 1024 + lane * 16)); }
constexpr unsigned OOB = 0x80000000u;

DEVI void glds16(const void* gsrc, unsigned lds_dst) {
  unsigned keep;
  asm volatile("s_mov_b32 %0, m0\n\ts_mov_b32 m0, %2\n\ts_nop 0\n\tglobal_load_lds_dwordx4 %1, off\n\ts_mov_b32 m0, %0" : "=&s"(keep) : "v"(gsrc), "s"(lds_dst) : "memory");
}
DEVI void drain() { asm volatile("s_waitcnt vmcnt(0)" ::: "memory"); }
DEVI void atom_add(char* ws, unsigned off, unsigned v) { __hip_atomic_fetch_add((unsigned*)(ws + off), v, __ATOMIC_RELAXED, __HIP_MEMORY_SCOPE_AGENT); }
DEVI void wait_flags(rsrc_t ws, unsigned off, unsigned target) {
  unsigned spins = 0;
  while (true) {
    asm volatile("" ::: "memory");
    unsigned v = ld1_sc1(ws, off);
    if (__all(v >= target) || spins > SPIN_CAP) break;
    ++spins;
    __builtin_amdgcn_s_sleep(2);
  }
  asm volatile("" ::: "memory");
}

DEVI void role_S(const Params& p, const int wv, int h, LAS char* smem, rsrc_t ws) {
  const int w = wv;
  if (w == 0) {
    int tid = wv * 64 + lane_id(); asm volatile("; roleSA" : "+v"(tid) :: "memory");

  const int lane = tid & 63;
  LAS char* wl = smem;
  LAS char* stg = smem + 122880;
  LAS char* ml = smem + 122880;
  LAS char* qk = ml + 10240;
  LAS char* kl = qk + 6144;
  LAS char* wlw = wl + (w - 1) * 40960;
  u32x4 pre[6][6];
  rsrc_t rq = __builtin_amdgcn_make_buffer_rsrc((void*)p.Wq, 0, 640 * 6144 * 2, 0x00020000);
  rsrc_t rk = __builtin_amdgcn_make_buffer_rsrc((void*)p.Wk, 0, 640 * 6144 * 2, 0x00020000);
  auto issue = [&](int it) {
    const int mat = it / 10, kk = it % 10;
#pragma unroll
    for (int i = 0; i < 6; i++) {
      int u = i * 256 + tid; int row = u / 24, pc = u - row * 24;
      pre[it % 6][i] = __builtin_amdgcn_raw_buffer_load_b128(mat ? rk : rq, row * 12288 + pc * 16, kk * 64 * 12288 + h * 384, 0);
    }
  };
  const int idx = (lane & 8) ? 0xEEEE : 0x4444;
  const int aoff = ((lane >> 3) & 1) * 512 + (lane >> 4) * 128 + (lane & 7) * 16;
  const float cs = 0.07216878364870322f * 1.4426950408889634f;
  int D = T_MEM * 4;
  unsigned tref = 0;
    bf16x16 W[3][10];
    issue(0); issue(1); issue(2); issue(3); issue(4); issue(5);
#pragma unroll
    for (int it = 0; it < 20; it++) {
      const int mat = it / 10, kk = it % 10;
#pragma unroll
      for (int i = 0; i < 6; i++) { const int u = i * 256 + tid, row = u / 24, pc = u - row * 24; *(LAS u32x4*)(stg + row * 400 + (row >> 5) * 32 + pc * 16) = pre[it % 6][i]; }
      __syncthreads();
      if (it + 6 < 20) issue(it + 6);
      if (mat == 0) {
#pragma unroll
        for (int t = 0; t < 3; t++) W[t][kk] = tr_frag(stg, 400, 16 * t, lane);
      }
      __syncthreads();
    }
    const unsigned eoff = ((lane & 15) < 8) ? (unsigned)((lane >> 4) * 128 + (lane & 7) * 16) : OOB;
    const unsigned poff = ((lane & 15) < 8 && lane < 32) ? (unsigned)((lane & 7) * 16 + (lane >> 4) * 8) : OOB;
    const unsigned aloff = (lane < 8) ? (unsigned)(lane * 16) : OOB;
    u32x4 kx[12];
    const unsigned kfoff = lane == 0 ? (unsigned)((32 + h) * 4) : OOB, kftgt = lane == 0 ? 6u : 0u;
    wait_flags(ws, kfoff, kftgt);
#pragma unroll
    for (int i = 0; i < 12; i++) kx[i] = ld_sc1(ws, OFF_KF + (0 * 32 + h) * 12288 + i * 1024 + lane * 16);
    const int g = lane >> 4, q4 = (lane & 15) >> 2, p4 = lane & 3;
    LAS char* qw = qk + (lane & 15) * 32 + (lane >> 4) * 8;
    for (int s = 0; s < NSEG; s++) {
      if (s > 0) wait_rt(tref + (D >> 2));
      bool fail = gather_mem(ws, s, ml + lane * 16, 0, lane * 16);
      if (s > 0) adapt(D, fail, T_MEM * 4);
      __syncthreads();
      unsigned long long tb1;
      asm volatile("s_memrealtime %0" : "=s"(tb1));
      AFr A; a_load(A, ml, aoff);
      f32x4 a0 = {0, 0, 0, 0}, a1 = {0, 0, 0, 0}, a2 = {0, 0, 0, 0};
      asm volatile("s_nop 1" : "+v"(a0), "+v"(a1), "+v"(a2));
#pragma unroll
      for (int kk = 0; kk < 10; kk++) {
        smf<true>(a0, A.a[kk], W[0][kk], idx); smf<true>(a1, A.a[kk], W[1][kk], idx); smf<true>(a2, A.a[kk], W[2][kk], idx);
        if (kk == 4) { asm volatile("s_waitcnt lgkmcnt(0)" : "+s"(tb1)); __builtin_amdgcn_raw_buffer_store_b32(san((unsigned)tb1), ws, lane == 0 ? 0u : OOB, OFF_TB + s * 512 + h * 16, 16); }
      }
      asm volatile("s_nop 7" : "+v"(a0), "+v"(a1), "+v"(a2));
      *(LAS u32x2*)(qw) = (u32x2){pk2(a0[0], a0[1]), pk2(a0[2], a0[3])};
      *(LAS u32x2*)(qw + 512) = (u32x2){pk2(a1[0], a1[1]), pk2(a1[2], a1[3])};
      *(LAS u32x2*)(qw + 1024) = (u32x2){pk2(a2[0], a2[1]), pk2(a2[2], a2[3])};
      __syncthreads();
      bf16x8 qf[6];
#pragma unroll
      for (int ks = 0; ks < 6; ks++) {
        int e0 = 32 * ks + 8 * g + q4;
        bf16x4 qa = trread(qk + e0 * 32 + p4 * 8), qb = trread(qk + (e0 + 4) * 32 + p4 * 8);
        qf[ks] = __builtin_shufflevector(qa, qb, 0, 1, 2, 3, 4, 5, 6, 7);
      }
      f32x4 T1, T2;
      asm volatile("s_nop 1\n\tv_mfma_f32_16x16x32_bf16 %0, %2, %4, 0\n\tv_mfma_f32_16x16x32_bf16 %1, %3, %4, 0" : "=&v"(T1), "=&v"(T2) : "v"(kx[0]), "v"(kx[1]), "v"(qf[0]));
#pragma unroll
      for (int ks = 1; ks < 6; ks++)
        asm volatile("v_mfma_f32_16x16x32_bf16 %0, %2, %4, %0\n\tv_mfma_f32_16x16x32_bf16 %1, %3, %4, %1" : "+v"(T1), "+v"(T2) : "v"(kx[2 * ks]), "v"(kx[2 * ks + 1]), "v"(qf[ks]));
      asm volatile("s_nop 7" : "+v"(T1), "+v"(T2));
#pragma unroll
      for (int r = 0; r < 4; r++) { T1[r] = ror8add(T1[r]); T2[r] = ror8add(T2[r]); }
      float mx = fmx3(fmx3(T1[0], T1[1], T1[2]), fmx3(T1[3], T2[0], T2[1]), fmx(T2[2], T2[3]));
      mx = xor32max(xor16max(mx));
      float E1[4], E2[4], lx = 0;
      {
        const f32x4 d1 = (T1 - mx) * cs, d2 = (T2 - mx) * cs;
        SB();
#pragma unroll
        for (int r = 0; r < 4; r++) { E1[r] = __builtin_amdgcn_exp2f(d1[r]); E2[r] = __builtin_amdgcn_exp2f(d2[r]); }
      }
      SB();
      {
        u32x4 ev = {san(pk2(E1[0], E1[1])), san(pk2(E1[2], E1[3])), san(pk2(E2[0], E2[1])), san(pk2(E2[2], E2[3]))};
        __builtin_amdgcn_raw_buffer_store_b128(ev, ws, eoff, OFF_EBUF + (s * 32 + h) * 512, 16);
      }
      lx = ((E1[0] + E1[1]) + (E1[2] + E1[3])) + ((E2[0] + E2[1]) + (E2[2] + E2[3]));
      lx = xor32add(xor16add(lx));
      __syncthreads();
      bf16x8 kmf[6];
#pragma unroll
      for (int ks = 0; ks < 6; ks++) {
        int e0 = 32 * ks + 8 * g + q4;
        bf16x4 ka = trread(kl + e0 * 16 + (p4 & 1) * 8), kb = trread(kl + (e0 + 4) * 16 + (p4 & 1) * 8);
        kmf[ks] = __builtin_shufflevector(ka, kb, 0, 1, 2, 3, 4, 5, 6, 7);
      }
      if (s == NSEG - 1) {
#pragma unroll
        for (int ks = 0; ks < 6; ks++) __builtin_amdgcn_raw_buffer_store_b128(__builtin_bit_cast(u32x4, kmf[ks]), ws, eoff, OFF_KM + ((s * 32 + h) * 6 + ks) * 512, 16);
        break;
      }
      f32x4 T0;
      asm volatile("s_nop 1\n\tv_mfma_f32_16x16x32_bf16 %0, %1, %2, 0" : "=&v"(T0) : "v"(kmf[0]), "v"(qf[0]));
#pragma unroll
      for (int ks = 1; ks < 6; ks++) asm volatile("v_mfma_f32_16x16x32_bf16 %0, %1, %2, %0" : "+v"(T0) : "v"(kmf[ks]), "v"(qf[ks]));
      asm volatile("s_nop 7" : "+v"(T0));
#pragma unroll
      for (int r = 0; r < 4; r++) T0[r] = ror8add(T0[r]);
      float mm = xor16max(fmx(fmx3(T0[0], T0[1], T0[2]), T0[3]));
      float m = fmx(mx, mm);
      const float ax = __builtin_amdgcn_exp2f((mx - m) * cs);
      float E0[4], lm;
      { const f32x4 d0 = (T0 - m) * cs;
#pragma unroll
        for (int r = 0; r < 4; r++) E0[r] = __builtin_amdgcn_exp2f(d0[r]); }
      lm = xor16add((E0[0] + E0[1]) + (E0[2] + E0[3]));
      float inv = __builtin_amdgcn_rcpf(lx * ax + lm);
      {
        u32x2 pv = {san(pk2(E0[0] * inv, E0[1] * inv)), san(pk2(E0[2] * inv, E0[3] * inv))};
        __builtin_amdgcn_raw_buffer_store_b64(pv, ws, poff, OFF_PMB + (s * 32 + h) * 128, 16);
        __builtin_amdgcn_raw_buffer_store_b32(san(__float_as_uint(ax * inv)), ws, aloff, OFF_ALB + s * 1024 + ((h >> 3) * 2 + (h & 1)) * 128 + ((h >> 1) & 3) * 4, 16);
      }
      tref = beacon_max(ws, s, lane, (unsigned)tb1);
#pragma unroll
      for (int ks = 0; ks < 6; ks++) __builtin_amdgcn_raw_buffer_store_b128(__builtin_bit_cast(u32x4, kmf[ks]), ws, eoff, OFF_KM + ((s * 32 + h) * 6 + ks) * 512, 16);
      if (s + 1 < NSEG) {
        wait_flags(ws, kfoff == OOB ? OOB : kfoff + (s + 1) * 512, kftgt);
        __builtin_amdgcn_raw_buffer_store_b32(1u, ws, lane == 0 ? 0u : OOB, OFF_KMF + (s * 32 + h) * 128, 16);
#pragma unroll
        for (int i = 0; i < 12; i++) kx[i] = ld_sc1(ws, OFF_KF + ((s + 1) * 32 + h) * 12288 + i * 1024 + lane * 16);
      }
    }
    drain(); __builtin_amdgcn_raw_buffer_store_b32(1u, ws, lane == 0 ? 0u : OOB, OFF_KMF + ((NSEG - 1) * 32 + h) * 128, 16);
    __builtin_amdgcn_endpgm(); __builtin_unreachable();
  } else {
    int tid = wv * 64 + lane_id(); asm volatile("; roleSB" : "+v"(tid) :: "memory");

  const int lane = tid & 63;
  LAS char* wl = smem;
  LAS char* stg = smem + 122880;
  LAS char* ml = smem + 122880;
  LAS char* qk = ml + 10240;
  LAS char* kl = qk + 6144;
  LAS char* wlw = wl + (w - 1) * 40960;
  u32x4 pre[6][6];
  rsrc_t rq = __builtin_amdgcn_make_buffer_rsrc((void*)p.Wq, 0, 640 * 6144 * 2, 0x00020000);
  rsrc_t rk = __builtin_amdgcn_make_buffer_rsrc((void*)p.Wk, 0, 640 * 6144 * 2, 0x00020000);
  auto issue = [&](int it) {
    const int mat = it / 10, kk = it % 10;
#pragma unroll
    for (int i = 0; i < 6; i++) {
      int u = i * 256 + tid; int row = u / 24, pc = u - row * 24;
      pre[it % 6][i] = __builtin_amdgcn_raw_buffer_load_b128(mat ? rk : rq, row * 12288 + pc * 16, kk * 64 * 12288 + h * 384, 0);
    }
  };
  const int idx = (lane & 8) ? 0xEEEE : 0x4444;
  const int aoff = ((lane >> 3) & 1) * 512 + (lane >> 4) * 128 + (lane & 7) * 16;
  const float cs = 0.07216878364870322f * 1.4426950408889634f;
  int D = T_MEM * 4;
  unsigned tref = 0;
    LAS char* qw = qk + (48 * w + (lane & 15)) * 32 + (lane >> 4) * 8;
    bf16x16 W[5][10];
    issue(0); issue(1); issue(2); issue(3); issue(4); issue(5);
#pragma unroll
    for (int it = 0; it < 20; it++) {
      const int mat = it / 10, kk = it % 10;
#pragma unroll
      for (int i = 0; i < 6; i++) { const int u = i * 256 + tid, row = u / 24, pc = u - row * 24; *(LAS u32x4*)(stg + row * 400 + (row >> 5) * 32 + pc * 16) = pre[it % 6][i]; }
      __syncthreads();
      if (it + 6 < 20) issue(it + 6);
      const int nt = mat == 0 ? 3 : 4;
#pragma unroll
      for (int t = 0; t < 4; t++) if (t < nt) {
        bf16x16 f = tr_frag(stg, 400, mat == 0 ? 16 * (3 * w + t) : 16 * (4 * (w - 1) + t), lane);
        if (t < nt - 1) W[mat == 0 ? t : 2 + t][kk] = f;
        else { LAS char* d = wlw + (mat * 10 + kk) * 2048 + lane * 16;
          *(LAS bf16x8*)d = __builtin_shufflevector(f, f, 0, 1, 2, 3, 4, 5, 6, 7);
          *(LAS bf16x8*)(d + 1024) = __builtin_shufflevector(f, f, 8, 9, 10, 11, 12, 13, 14, 15); }
      }
      __syncthreads();
    }
    LAS char* k01addr = kl + (64 * (w - 1) + 16 * (lane >> 5) + (lane & 15)) * 16 + ((lane >> 4) & 1) * 8;
    for (int s = 0; s < NSEG; s++) {
      if (s > 0) wait_rt(tref + (D >> 2));
      bf16x16 fb[4];
#pragma unroll
      for (int q = 0; q < 3; q++) fb[q] = fb_load(wlw, q, lane);
      bool fail = gather_mem(ws, s, ml + w * 1024 + lane * 16, w, w * 1024 + lane * 16);
      if (s > 0) adapt(D, fail, T_MEM * 4);
      __syncthreads();
      AFr A; a_load(A, ml, aoff);
      f32x4 a0 = {0, 0, 0, 0}, a1 = {0, 0, 0, 0}, a2 = {0, 0, 0, 0};
      asm volatile("s_nop 1" : "+v"(a0), "+v"(a1), "+v"(a2));
#pragma unroll
      for (int kk = 0; kk < 10; kk++) {
        smf<true>(a0, A.a[kk], W[0][kk], idx);
        SB();
        bf16x8 lo = *(LAS bf16x8*)(wlw + (kk + 3) * 2048 + lane * 16);
        SB();
        if (10 + kk < 32) smf<true>(a1, A.a[kk], W[1][kk], idx); else smf<false>(a1, A.a[kk], W[1][kk], idx);
        SB();
        bf16x8 hi = *(LAS bf16x8*)(wlw + (kk + 3) * 2048 + 1024 + lane * 16);
        SB();
        smf<false>(a2, A.a[kk], fb[kk % 4], idx);
        fb[(kk + 3) % 4] = cat16(lo, hi);
        SB();
      }
      asm volatile("s_nop 7" : "+v"(a0), "+v"(a1), "+v"(a2));
      *(LAS u32x2*)(qw) = (u32x2){pk2(a0[0], a0[1]), pk2(a0[2], a0[3])};
      *(LAS u32x2*)(qw + 512) = (u32x2){pk2(a1[0], a1[1]), pk2(a1[2], a1[3])};
      *(LAS u32x2*)(qw + 1024) = (u32x2){pk2(a2[0], a2[1]), pk2(a2[2], a2[3])};
      SB();
      f32x4 y0 = {0, 0, 0, 0}, y1 = {0, 0, 0, 0}, x0 = {0, 0, 0, 0}, x1 = {0, 0, 0, 0};
      asm volatile("s_nop 1" : "+v"(y0), "+v"(y1), "+v"(x0), "+v"(x1));
#pragma unroll
      for (int kk = 0; kk < 10; kk++) {
        smf<false>(y0, A.a[kk], W[4][kk], idx);
        SB();
        bf16x8 lo, hi;
        if (kk + 3 < 10) lo = *(LAS bf16x8*)(wlw + (kk + 13) * 2048 + lane * 16);
        SB();
        smf<true>(x0, A.a[kk], W[2][kk], idx);
        SB();
        if (kk + 3 < 10) hi = *(LAS bf16x8*)(wlw + (kk + 13) * 2048 + 1024 + lane * 16);
        SB();
        if (kk < 2) smf<true>(x1, A.a[kk], W[3][kk], idx); else smf<false>(x1, A.a[kk], W[3][kk], idx);
        SB();
        smf<false>(y1, A.a[kk], fb[(kk + 10) % 4], idx);
        if (kk + 3 < 10) fb[(kk + 13) % 4] = cat16(lo, hi);
        SB();
        if (kk == 0) { asm volatile("s_waitcnt lgkmcnt(2)\n\ts_barrier" ::: "memory"); SB(); }
      }
      asm volatile("s_nop 7" : "+v"(x0), "+v"(x1), "+v"(y0), "+v"(y1));
      {
        f32x4 o = halfsum2(x0, x1), oy = halfsum2(y0, y1);
        *(LAS u32x2*)(k01addr) = (u32x2){pk2(o[0], o[1]), pk2(o[2], o[3])};
        *(LAS u32x2*)(k01addr + 512) = (u32x2){pk2(oy[0], oy[1]), pk2(oy[2], oy[3])};
      }
      __syncthreads();
      tref = beacon_max(ws, s, lane, rt32());
    }
    __builtin_amdgcn_endpgm(); __builtin_unreachable();
  }
}

DEVI void role_V(const Params& p, const int wv, int j, LAS char* smem, rsrc_t ws) {
  int tid = wv * 64 + lane_id(); asm volatile("; roleV" : "+v"(tid) :: "memory");
  const int lane = tid & 63, w = __builtin_amdgcn_readfirstlane(tid >> 6);
  LAS char* ml = smem;
  LAS char* yl = smem + 10240;
  LAS char* red = smem + 10240 + 4096;
  bf16x16 B[4][10];
  wait_flags(ws, lane == 0 ? (unsigned)(OFF_BRDY + (j / 20) * 128) : OOB, lane == 0 ? 32u : 0u);
#pragma unroll
  for (int t = 0; t < 4; t++)
#pragma unroll
    for (int kk = 0; kk < 10; kk++) {
      unsigned o = OFF_BF + ((j * 16 + 4 * w + t) * 10 + kk) * 2048 + lane * 16;
      B[t][kk] = cat16(__builtin_bit_cast(bf16x8, ld_sc1(ws, o)), __builtin_bit_cast(bf16x8, ld_sc1(ws, o + 1024)));
    }
  const int idx = (lane & 8) ? 0xEEEE : 0x4444;
  const int half = (lane >> 3) & 1, g = lane >> 4, i8 = lane & 7;
  const int aoff = half * 512 + g * 128 + i8 * 16;
  const unsigned moff = ((lane & 15) < 8) ? (unsigned)(i8 * 16 + ((lane >> 4) & 1) * 8 + (lane >> 5) * 4) : OOB;
  const unsigned evo = (8 * w + half) * 512 + g * 128 + i8 * 16;
  const unsigned avo = (w * 2 + (g >> 1)) * 128 + i8 * 16;
  const unsigned pvo = (8 * w + 4 * half + g) * 128 + i8 * 16;
  const unsigned yvo = (j * 32 + 8 * w + (lane >> 3)) * 128 + i8 * 16;
  const unsigned zvo = ((j * 32 + 8 * w + half) * 5 + 1 + g) * 128 + i8 * 16;
  u32x4 zx[4];
  auto zload = [&](int s) {
#pragma unroll
    for (int q = 0; q < 4; q++) zx[q] = ldx(ws, zvo + q * 1280, OFF_Z + s * (80 * 20480));
  };
  const unsigned vfoff = lane == 0 ? (unsigned)(OFF_REC + 0) : OOB, vftgt = lane == 0 ? 32u : 0u;
  int Dm = T_MEM * 4, De = T_E * 4, Dl = T_L * 4;
  unsigned tb = 0;
  unsigned vfl = 0;
  for (int s = 0; s < NSEG; s++) {
    if (s > 0) wait_rt(tb + (Dm >> 2));
    bool failm = gather_mem(ws, s, ml + w * 1024 + lane * 16, w, w * 1024 + lane * 16);
    if (!__all(vfl >= vftgt)) wait_flags(ws, vfoff == OOB ? OOB : vfoff + s * 128, vftgt);
    zload(s);
    if (s > 0) adapt(Dm, failm, T_MEM * 4);
    __syncthreads();
    f32x4 acc[4];
    {
      AFr A; a_load(A, ml, aoff);
#pragma unroll
      for (int t = 0; t < 4; t++) acc[t] = (f32x4){0, 0, 0, 0};
      asm volatile("s_nop 1" : "+v"(acc[0]), "+v"(acc[1]), "+v"(acc[2]), "+v"(acc[3]));
#pragma unroll
      for (int kk = 0; kk < 10; kk++)
#pragma unroll
        for (int t = 0; t < 4; t++) { if (t * 10 + kk < 32) smf<true>(acc[t], A.a[kk], B[t][kk], idx); else smf<false>(acc[t], A.a[kk], B[t][kk], idx); }
      asm volatile("s_nop 7" : "+v"(acc[0]), "+v"(acc[1]), "+v"(acc[2]), "+v"(acc[3]));
    }
#pragma unroll
    for (int pr = 0; pr < 2; pr++) {
      f32x4 o = halfsum2(acc[2 * pr], acc[2 * pr + 1]);
      int t = 2 * pr + (lane >> 5);
      int hl = 2 * t + ((lane >> 3) & 1);
      u32x2 v = {pk2(o[0], o[1]), pk2(o[2], o[3])};
      *(LAS u32x2*)(yl + w * 1024 + (hl * 8 + i8) * 16 + ((lane >> 4) & 1) * 8) = v;
    }
    bf16x8 yf = *(LAS bf16x8*)(yl + w * 1024 + ((4 * half + g) * 8 + i8) * 16);
    u32x4 yv = *(LAS u32x4*)(yl + w * 1024 + lane * 16);
    if (s == NSEG - 1) {
      __builtin_amdgcn_raw_buffer_store_b128(yv, ws, yvo, OFF_YB + s * (80 * 4096), 16);
      drain(); __syncthreads();
      if (w == 1) __builtin_amdgcn_raw_buffer_store_b32(1u, ws, lane == 0 ? (unsigned)(j * 4) : OOB, OFF_YF + s * 512, 16);
      break;
    }
    f32x4 ox[4]; bool fe_ = false, fl_ = false;
    {
      u32x4 ev[4]; u32x4 al; u32x4 pm;
      const unsigned se = OFF_EBUF + s * 16384, sa = OFF_ALB + s * 1024, sp = OFF_PMB + s * 4096;
      auto ld_e = [&]() {
#pragma unroll
        for (int q = 0; q < 4; q++) ev[q] = ldx(ws, evo + q * 1024, se);
      };
      auto ld_l = [&]() { al = ldx(ws, avo, sa); pm = ldx(ws, pvo, sp); };
      tb = beacon_max(ws, s, lane, rt32());
      __builtin_amdgcn_raw_buffer_store_b128(yv, ws, yvo, OFF_YB + s * (80 * 4096), 16);
      const unsigned te = tb + (De >> 2), tl = tb + (Dl >> 2);
      SB();
      wait_rt(te);
      ld_e();
      vfl = ld1_sc1(ws, (vfoff == OOB || s + 1 >= NSEG) ? OOB : vfoff + (s + 1) * 128);
      SB();
      wait_rt(tl);
      ld_l();
      SB();
      bool fe = false, fl = false;
      if (has_sent(max(max(mx4(ev[0]), mx4(ev[1])), max(mx4(ev[2]), mx4(ev[3]))))) {
        fe = true;
        for (unsigned spins = 0; spins < SPIN_CAP; spins++) {
          asm volatile("" ::: "memory");
          ld_e();
          SB();
          if (!has_sent(max(max(mx4(ev[0]), mx4(ev[1])), max(mx4(ev[2]), mx4(ev[3]))))) break;
        }
      }
      asm volatile("s_nop 1\n\tv_mfma_f32_16x16x32_bf16 %0, %4, %8, 0\n\tv_mfma_f32_16x16x32_bf16 %1, %5, %9, 0\n\tv_mfma_f32_16x16x32_bf16 %2, %6, %10, 0\n\tv_mfma_f32_16x16x32_bf16 %3, %7, %11, 0"
                   : "=&v"(ox[0]), "=&v"(ox[1]), "=&v"(ox[2]), "=&v"(ox[3]) : "v"(zx[0]), "v"(zx[1]), "v"(zx[2]), "v"(zx[3]), "v"(ev[0]), "v"(ev[1]), "v"(ev[2]), "v"(ev[3]));
      SB();
      float s01, s23;
      auto late = [&]() {
        f32x4 tm4;
        asm volatile("s_nop 1\n\tv_mfma_f32_16x16x32_bf16 %0, %1, %2, 0" : "=&v"(tm4) : "v"(yf), "v"(pm));
        asm volatile("s_nop 3" : "+v"(ox[0]), "+v"(ox[1]), "+v"(ox[2]), "+v"(ox[3]));
        const float a0 = __uint_as_float(al.x), a1 = __uint_as_float(al.y), a2 = __uint_as_float(al.z), a3 = __uint_as_float(al.w);
        f32x4 u0 = ox[0] * a0, u1 = ox[2] * a2;
        u0 = __builtin_elementwise_fma(ox[1], (f32x4){a1, a1, a1, a1}, u0); u1 = __builtin_elementwise_fma(ox[3], (f32x4){a3, a3, a3, a3}, u1);
        u0 += u1;
        asm volatile("" : "+v"(tm4), "+v"(u0));
        u0 += tm4;
        float t[4] = {u0[0], u0[1], u0[2], u0[3]};
        auto a = __builtin_amdgcn_permlane32_swap(__float_as_uint(t[0]), __float_as_uint(t[1]), false, false);
        auto b = __builtin_amdgcn_permlane32_swap(__float_as_uint(t[2]), __float_as_uint(t[3]), false, false);
        unsigned a0_ = a[0], a1_ = a[1], b0_ = b[0], b1_ = b[1];
        s01 = __uint_as_float(a0_) + __uint_as_float(__builtin_amdgcn_update_dpp(0u, a1_, 0x128, 0xf, 0xf, false));
        s23 = __uint_as_float(b0_) + __uint_as_float(__builtin_amdgcn_update_dpp(0u, b1_, 0x128, 0xf, 0xf, false));
      };
      late();
      if (has_sent(max(mx4(al), mx4(pm)))) {
        fl = true;
        for (unsigned spins = 0; spins < SPIN_CAP; spins++) {
          asm volatile("" ::: "memory");
          ld_l();
          SB();
          if (!has_sent(max(mx4(al), mx4(pm)))) break;
        }
        late();
      }
      fe_ = fe; fl_ = fl;
      if (w != 0) *(LAS f32x2*)(red + w * 512 + lane * 8) = (f32x2){s01, s23};
      __syncthreads();
      if (w == 0) {
        f32x2 r1 = *(LAS f32x2*)(red + 512 + lane * 8), r2 = *(LAS f32x2*)(red + 1024 + lane * 8), r3 = *(LAS f32x2*)(red + 1536 + lane * 8);
        const float u01 = (s01 + r1[0]) + (r2[0] + r3[0]), u23 = (s23 + r1[1]) + (r2[1] + r3[1]);
        auto c = __builtin_amdgcn_permlane32_swap(__float_as_uint(u01), __float_as_uint(u23), false, false);
        unsigned c0 = c[0], c1 = c[1];
        __builtin_amdgcn_raw_buffer_store_b32(san(pk2(__uint_as_float(c0), __uint_as_float(c1))), ws, moff, OFF_MEMB + (s + 1) * 10240 + j * 128, 16);
      }
    }
    adapt(De, fe_, T_E * 4); adapt(Dl, fl_, T_L * 4);
  }
  __builtin_amdgcn_endpgm(); __builtin_unreachable();
}

template <bool AG> DEVI void mf0(f32x4& acc, const bf16x8& a, const bf16x8& b) {
  if constexpr (AG) asm volatile("v_mfma_f32_16x16x32_bf16 %0, %1, %2, 0" : "=&v"(acc) : "a"(a), "v"(b));
  else asm volatile("v_mfma_f32_16x16x32_bf16 %0, %1, %2, 0" : "=&v"(acc) : "v"(a), "v"(b));
}
template <bool AG> DEVI void mfa(f32x4& acc, const bf16x8& a, const bf16x8& b) {
  if constexpr (AG) asm volatile("v_mfma_f32_16x16x32_bf16 %0, %1, %2, %0" : "+v"(acc) : "a"(a), "v"(b));
  else asm volatile("v_mfma_f32_16x16x32_bf16 %0, %1, %2, %0" : "+v"(acc) : "v"(a), "v"(b));
}
template <bool AG> DEVI void mf0B(f32x4& acc, const bf16x8& a, const bf16x8& b) {
  if constexpr (AG) asm volatile("v_mfma_f32_16x16x32_bf16 %0, %1, %2, 0" : "=&v"(acc) : "v"(a), "a"(b));
  else asm volatile("v_mfma_f32_16x16x32_bf16 %0, %1, %2, 0" : "=&v"(acc) : "v"(a), "v"(b));
}
template <bool AG> DEVI void mfaB(f32x4& acc, const bf16x8& a, const bf16x8& b) {
  if constexpr (AG) asm volatile("v_mfma_f32_16x16x32_bf16 %0, %1, %2, %0" : "+v"(acc) : "v"(a), "a"(b));
  else asm volatile("v_mfma_f32_16x16x32_bf16 %0, %1, %2, %0" : "+v"(acc) : "v"(a), "v"(b));
}
DEVI void wait_flags_slow(rsrc_t ws, unsigned off, unsigned target, bool fast = false) {
  unsigned spins = 0;
  while (true) {
    asm volatile("" ::: "memory");
    unsigned v = ld1_sc1(ws, off);
    if (__all(v >= target) || spins > SPIN_CAP) break;
    ++spins;
    if (fast) __builtin_amdgcn_s_sleep(2); else __builtin_amdgcn_s_sleep(12);
  }
  asm volatile("" ::: "memory");
}
DEVI bf16x8 tr8(LAS char* tile, int ldb, int row0, int colbyte, int lane) {
  const int g = lane >> 4, q4 = (lane & 15) >> 2;
  bf16x4 a = trread(tile + (row0 + 8 * g + q4) * ldb + colbyte), b = trread(tile + (row0 + 8 * g + 4 + q4) * ldb + colbyte);
  return __builtin_shufflevector(a, b, 0, 1, 2, 3, 4, 5, 6, 7);
}
DEVI u32x4 pack8(const f32x4& a, const f32x4& b) { return (u32x4){pk2(a[0], a[1]), pk2(a[2], a[3]), pk2(b[0], b[1]), pk2(b[2], b[3])}; }
DEVI float rbf(float v) { return (float)(__bf16)v; }

DEVI void x_dma(const Params& p, int s, unsigned ldsbase, int w, int lane) {
  const char* src = p.ws + OFF_XNF + s * 40960 + w * 10240 + lane * 16;
#pragma unroll
  for (int i = 0; i < 10; i++) glds16(src + i * 1024, ldsbase + w * 10240 + i * 1024);
}

template <int NT, int NKS, class F>
DEVI void dense_phase(f32x4 (&acc)[NT][2], const bf16x8 (&W)[NT][NKS], LAS char* xb, int lane, F filler) {
  bf16x8 b[3][2];
#pragma unroll
  for (int q = 0; q < 2; q++) { b[q][0] = *(LAS bf16x8*)(xb + (q * 2) * 1024 + lane * 16); b[q][1] = *(LAS bf16x8*)(xb + (q * 2 + 1) * 1024 + lane * 16); }
#pragma unroll
  for (int kk = 0; kk < NKS; kk++) {
#pragma unroll
    for (int t = 0; t < NT; t++)
#pragma unroll
      for (int tt = 0; tt < 2; tt++) {
        if (kk == 0) { if (t * NKS + kk < 64) mf0<true>(acc[t][tt], W[t][kk], b[kk % 3][tt]); else mf0<false>(acc[t][tt], W[t][kk], b[kk % 3][tt]); }
        else { if (t * NKS + kk < 64) mfa<true>(acc[t][tt], W[t][kk], b[kk % 3][tt]); else mfa<false>(acc[t][tt], W[t][kk], b[kk % 3][tt]); }
        SB();
        if (t == 0 && tt == 1 && kk + 2 < NKS) b[(kk + 2) % 3][0] = *(LAS bf16x8*)(xb + ((kk + 2) * 2) * 1024 + lane * 16);
        if (t == 1 && tt == 1 && kk + 2 < NKS) b[(kk + 2) % 3][1] = *(LAS bf16x8*)(xb + ((kk + 2) * 2 + 1) * 1024 + lane * 16);
        if (t == 2 && tt == 1) filler(kk);
        SB();
      }
  }
  asm volatile("s_nop 7" ::: "memory");
#pragma unroll
  for (int t = 0; t < NT; t++) asm volatile("" : "+v"(acc[t][0]), "+v"(acc[t][1]));
}

DEVI void role_QKV(const Params& p, const int wv, int c, LAS char* smem, rsrc_t ws) {
  int tid = wv * 64 + lane_id(); asm volatile("; roleQKV" : "+v"(tid) :: "memory");
  const int lane = tid & 63, w = __builtin_amdgcn_readfirstlane(tid >> 6);
  const unsigned lds0 = (unsigned)(size_t)smem;
  bf16x8 W[5][20];
  x_dma(p, 0, lds0 + 81920, w, lane);
  {
    const int pc = tid % 40, r0 = min(tid / 40, 5);
    const int gcol = min(320 * c + 8 * pc, 18432 - 8);
    const int mat = gcol / 6144, cm = gcol - mat * 6144;
    const uint16_t* base = (mat == 0 ? p.Wq : mat == 1 ? p.Wk : p.Wv) + cm;
    u32x4 pre[3][11];
    auto issue = [&](int ch) {
#pragma unroll
      for (int i = 0; i < 11; i++) { int r = min(r0 + 6 * i, 63); pre[ch % 3][i] = *(const u32x4*)(base + (size_t)(64 * ch + r) * 6144); }
    };
    issue(0); issue(1); issue(2);
    const int p4 = lane & 3;
#pragma unroll
    for (int ch = 0; ch < 10; ch++) {
#pragma unroll
      for (int i = 0; i < 11; i++) { int r = min(r0 + 6 * i, 63); *(LAS u32x4*)(smem + r * 656 + pc * 16) = pre[ch % 3][i]; }
      __syncthreads();
      if (ch + 3 < 10) issue(ch + 3);
#pragma unroll
      for (int k2 = 0; k2 < 2; k2++)
#pragma unroll
        for (int t = 0; t < 5; t++) {
          const int blk = t < 4 ? 2 * w + (t >> 1) : 8 + (w >> 1), u = t < 4 ? (t & 1) : (w & 1);
          W[t][2 * ch + k2] = tr8(smem, 656, 32 * k2, (32 * blk + 8 * p4 + 4 * u) * 2, lane);
        }
      __syncthreads();
    }
  }
  const int nb = min(10, 576 - 10 * c);
  unsigned dst[3];
#pragma unroll
  for (int q = 0; q < 3; q++) {
    int blk = q < 2 ? 2 * w + q : 8 + (w >> 1);
    int gb = 10 * c + blk; int mat = gb / 192, bb = gb - mat * 192;
    dst[q] = blk < nb ? OFF_QF + mat * SZ_QF + bb * 2048 + lane * 16 + (q == 2 ? (w & 1) * 8 : 0) : OOB;
  }
  const int g0 = (10 * c) / 6;
  const int gid = g0 + lane; const int lo = max(10 * c, 6 * gid), hi = min(10 * c + nb, 6 * gid + 6);
  drain(); __syncthreads();
  for (int s = 0; s < NSEG; s++) {
    LAS char* xb = smem + (((s + 1) & 1) ? 81920 : 0);
    const int sn = s + 1 < NSEG ? s + 1 : s;
    const char* src = p.ws + OFF_XNF + sn * 40960 + w * 10240 + lane * 16;
    const unsigned ldst = lds0 + ((s & 1) ? 81920 : 0) + w * 10240;
    f32x4 acc[5][2];
    dense_phase<5, 20>(acc, W, xb, lane, [&](int kk) { if (kk < 10) glds16(src + kk * 1024, ldst + kk * 1024); });
    drain(); __syncthreads();
    if (s > 4 && w == 0 && lane < 3 && hi > lo) atom_add(p.ws, ((s - 1) * 128 + gid) * 4, hi - lo);
#pragma unroll
    for (int tt = 0; tt < 2; tt++) {
      const unsigned so = s * 192 * 2048 + tt * 1024;
      __builtin_amdgcn_raw_buffer_store_b128(pack8(acc[0][tt], acc[1][tt]), ws, dst[0], so, 16);
      __builtin_amdgcn_raw_buffer_store_b128(pack8(acc[2][tt], acc[3][tt]), ws, dst[1], so, 16);
      __builtin_amdgcn_raw_buffer_store_b64((u32x2){pk2(acc[4][tt][0], acc[4][tt][1]), pk2(acc[4][tt][2], acc[4][tt][3])}, ws, dst[2], so, 16);
    }
    if (s < 4) { drain(); __syncthreads(); if (w == 0 && lane < 3 && hi > lo) atom_add(p.ws, (s * 128 + gid) * 4, hi - lo); }
  }
  drain(); __syncthreads();
  if (w == 0 && lane < 3 && hi > lo) atom_add(p.ws, ((NSEG - 1) * 128 + gid) * 4, hi - lo);

}

DEVI void role_VO(const Params& p, const int wv, int h, LAS char* smem, rsrc_t ws) {
  int tid = wv * 64 + lane_id(); asm volatile("; roleVO" : "+v"(tid) :: "memory");
  const int lane = tid & 63, w = __builtin_amdgcn_readfirstlane(tid >> 6);
  bf16x8 W[10][6];
  {
    rsrc_t ro = __builtin_amdgcn_make_buffer_rsrc((void*)(p.Wo + (size_t)h * 192 * 640), 0, 192 * 640 * 2, 0x00020000);
    u32x4 pre[6][10];
    auto issue = [&](int ch) {
#pragma unroll
      for (int i = 0; i < 10; i++) pre[ch][i] = __builtin_amdgcn_raw_buffer_load_b128(ro, (i * 256 + tid) * 16, ch * 40960, 0);
    };
    issue(0); issue(1); issue(2); issue(3); issue(4); issue(5);
    const int p4 = lane & 3;
#pragma unroll
    for (int ch = 0; ch < 6; ch++) {
#pragma unroll
      for (int i = 0; i < 10; i++) { const int u = i * 256 + tid, row = u / 80, pc = u - row * 80; *(LAS u32x4*)(smem + row * 1328 + pc * 16) = pre[ch][i]; }
      __syncthreads();
#pragma unroll
      for (int t = 0; t < 10; t++) W[t][ch] = tr8(smem, 1328, 0, (16 * (10 * w + t) + 4 * p4) * 2, lane);
      __syncthreads();
    }
  }
  const int n = lane & 15, g = lane >> 4;
  const unsigned foff = lane == 0 ? (unsigned)((64 + h) * 4) : OOB;
  const unsigned ftgt = lane == 0 ? 6u : 0u;
  const unsigned zl = ((((20 * w + (n >> 3)) * 32 + h) * 5 + 1 + g) * 128 + (n & 7) * 16);
  u32x4 st[3];
  auto vload = [&](int s) {
#pragma unroll
    for (int i = 0; i < 3; i++) st[i] = ldx(ws, (i * 256 + tid) * 16, OFF_VF + (s * 192 + h * 6) * 2048);
  };
  auto vput = [&](int s) {
#pragma unroll
    for (int i = 0; i < 3; i++) *(LAS u32x4*)(smem + (s & 1) * 12288 + (i * 256 + tid) * 16) = st[i];
  };
  auto half = [&](f32x4 (&acc)[5][2], const bf16x8 (&d)[2][6], auto HF) {
    constexpr int hf = decltype(HF)::value;
#pragma unroll
    for (int ks = 0; ks < 6; ks++)
#pragma unroll
      for (int t = 0; t < 5; t++)
#pragma unroll
        for (int mt = 0; mt < 2; mt++) {
          if (ks == 0) mf0B<true>(acc[t][mt], d[mt][ks], W[5 * hf + t][ks]); else mfaB<true>(acc[t][mt], d[mt][ks], W[5 * hf + t][ks]);
        }
  };
  auto store = [&](f32x4 (&acc)[5][2], int s, int hf) {
#pragma unroll
    for (int t = 0; t < 5; t++) {
      asm volatile("" : "+v"(acc[t][0]), "+v"(acc[t][1]));
      __builtin_amdgcn_raw_buffer_store_b128(pack8(acc[t][0], acc[t][1]), ws, zl + (5 * hf + t) * 2 * 20480, OFF_Z + s * 80 * 20480, 16);
    }
  };
  wait_flags(ws, foff, ftgt);
  vload(0); vput(0);
  __syncthreads();
  for (int s = 0; s < NSEG; s++) {
    const int sn = s + 1 < NSEG ? s + 1 : s;
    const unsigned fo = foff == OOB ? OOB : foff + sn * 512;
    unsigned fl = ld1_sc1(ws, fo);
    bf16x8 cur[2][6];
#pragma unroll
    for (int ks = 0; ks < 6; ks++)
#pragma unroll
      for (int mt = 0; mt < 2; mt++) cur[mt][ks] = *(LAS bf16x8*)(smem + (s & 1) * 12288 + (ks * 2 + mt) * 1024 + lane * 16);
    SB();
    f32x4 acc0[5][2], acc1[5][2];
    half(acc0, cur, std::integral_constant<int, 0>());
    SB();
    drain(); __syncthreads();
    if (s > 3 && tid == 0) atom_add(p.ws, OFF_REC + (s - 1) * 128 + 0, 1);
    const bool vready = __all(fl >= ftgt);
    if (vready) vload(sn);
    store(acc0, s, 0);
    SB();
    half(acc1, cur, std::integral_constant<int, 1>());
    asm volatile("s_nop 7" ::: "memory");
    store(acc1, s, 1);
    if (s < 3) { drain(); __syncthreads(); if (tid == 0) atom_add(p.ws, OFF_REC + s * 128 + 0, 1); }
    if (!vready) { wait_flags(ws, fo, ftgt); vload(sn); }
    vput(sn + (s + 1 < NSEG ? 0 : 1));
    __syncthreads();
  }
  drain(); __syncthreads();
  if (tid == 0) atom_add(p.ws, OFF_REC + (NSEG - 1) * 128 + 0, 1);

}

DEVI void role_GU(const Params& p, const int wv, int c, LAS char* smem, rsrc_t ws) {
  int tid = wv * 64 + lane_id(); asm volatile("; roleGU" : "+v"(tid) :: "memory");
  const int lane = tid & 63, w = __builtin_amdgcn_readfirstlane(tid >> 6);
  const unsigned lds0 = (unsigned)(size_t)smem;
  bf16x8 W[4][20];
  x_dma(p, 0, lds0 + 81920, w, lane);
  {
    rsrc_t rg = __builtin_amdgcn_make_buffer_rsrc((void*)p.Wg, 0, 640 * 1536 * 2, 0x00020000);
    rsrc_t ru = __builtin_amdgcn_make_buffer_rsrc((void*)p.Wu, 0, 640 * 1536 * 2, 0x00020000);
    u32x4 pre[5][8];
    auto issue = [&](int ch) {
#pragma unroll
      for (int i = 0; i < 8; i++) { int u = i * 256 + tid; int row = (u >> 4) & 63, pc = u & 15;
        pre[ch % 5][i] = __builtin_amdgcn_raw_buffer_load_b128(i < 4 ? rg : ru, row * 3072 + pc * 16, ch * 64 * 3072 + c * 256, 0); }
    };
    issue(0); issue(1); issue(2); issue(3); issue(4);
    const int p4 = lane & 3;
#pragma unroll
    for (int ch = 0; ch < 10; ch++) {
#pragma unroll
      for (int i = 0; i < 8; i++) { const int u = i * 256 + tid; *(LAS u32x4*)(smem + (u >> 4) * 304 + (u & 15) * 16) = pre[ch % 5][i]; }
      __syncthreads();
      if (ch + 5 < 10) issue(ch + 5);
#pragma unroll
      for (int k2 = 0; k2 < 2; k2++)
#pragma unroll
        for (int t = 0; t < 4; t++) W[t][2 * ch + k2] = tr8(smem + (t >> 1) * (64 * 304), 304, 32 * k2, (32 * w + 8 * p4 + 4 * (t & 1)) * 2, lane);
      __syncthreads();
    }
  }
  drain(); __syncthreads();
  for (int s = 0; s < NSEG; s++) {
    LAS char* xb = smem + (((s + 1) & 1) ? 81920 : 0);
    const int sn = s + 1 < NSEG ? s + 1 : s;
    const char* src = p.ws + OFF_XNF + sn * 40960 + w * 10240 + lane * 16;
    const unsigned ldst = lds0 + ((s & 1) ? 81920 : 0) + w * 10240;
    f32x4 acc[4][2];
    dense_phase<4, 20>(acc, W, xb, lane, [&](int kk) { if (kk < 10) glds16(src + kk * 1024, ldst + kk * 1024); });
    drain(); __syncthreads();
    if (s > 0 && tid == 0) atom_add(p.ws, OFF_CH + (s - 1) * 128, 1);
#pragma unroll
    for (int tt = 0; tt < 2; tt++) {
      f32x4 h0, h1;
#pragma unroll
      for (int r = 0; r < 4; r++) { h0[r] = rbf(acc[0][tt][r]) * rbf(acc[2][tt][r]); h1[r] = rbf(acc[1][tt][r]) * rbf(acc[3][tt][r]); }
      __builtin_amdgcn_raw_buffer_store_b128(pack8(h0, h1), ws, lane * 16, OFF_HF + ((s * 48 + 4 * c + w) * 2 + tt) * 1024, 16);
    }
  }
  drain(); __syncthreads();
  if (tid == 0) atom_add(p.ws, OFF_CH + (NSEG - 1) * 128, 1);

}

DEVI void role_DN(const Params& p, const int wv, int cc, LAS char* smem, rsrc_t ws) {
  const int c = cc % 5, par = cc / 5;
  int tid = wv * 64 + lane_id(); asm volatile("; roleDN" : "+v"(tid) :: "memory");
  const int lane = tid & 63, w = __builtin_amdgcn_readfirstlane(tid >> 6);
  bf16x8 W[2][48];
  {
    rsrc_t rd = __builtin_amdgcn_make_buffer_rsrc((void*)p.Wd, 0, 1536 * 640 * 2, 0x00020000);
    u32x4 pre[3][8];
    auto issue = [&](int ch) {
#pragma unroll
      for (int i = 0; i < 8; i++) { int u = i * 256 + tid; int row = u >> 4, pc = u & 15;
        pre[ch % 3][i] = __builtin_amdgcn_raw_buffer_load_b128(rd, row * 1280 + pc * 16, ch * 128 * 1280 + c * 256, 0); }
    };
    issue(0); issue(1); issue(2);
    const int p4 = lane & 3;
#pragma unroll
    for (int ch = 0; ch < 12; ch++) {
#pragma unroll
      for (int i = 0; i < 8; i++) { const int u = i * 256 + tid; *(LAS u32x4*)(smem + (u >> 4) * 304 + (u & 15) * 16) = pre[ch % 3][i]; }
      __syncthreads();
      if (ch + 3 < 12) issue(ch + 3);
#pragma unroll
      for (int k4 = 0; k4 < 4; k4++)
#pragma unroll
        for (int t = 0; t < 2; t++) W[t][4 * ch + k4] = tr8(smem, 304, 32 * k4, (32 * w + 8 * p4 + 4 * t) * 2, lane);
      __syncthreads();
    }
  }
  const unsigned foff = lane == 0 ? (unsigned)(OFF_CH) : OOB;
  const unsigned ftgt = lane == 0 ? 12u : 0u;
  const int n = lane & 15, g = lane >> 4;
  u32x4 st[12];
  auto hload = [&](int s, int hf) {
#pragma unroll
    for (int i = 0; i < 12; i++) st[i] = __builtin_amdgcn_raw_buffer_load_b128(ws, (i * 256 + tid) * 16, OFF_HF + s * 98304 + hf * 49152, 16);
  };
  wait_flags(ws, foff == OOB ? OOB : foff + par * 128, ftgt);
  hload(par, 0);
  for (int s = par; s < NSEG; s += 2) {
    f32x4 acc[2][2];
#pragma unroll
    for (int hf = 0; hf < 2; hf++) {
#pragma unroll
      for (int i = 0; i < 12; i++) *(LAS u32x4*)(smem + hf * 49152 + (i * 256 + tid) * 16) = st[i];
      if (hf == 0) drain();
      __syncthreads();
      if (hf == 0) { if (s > 1 && tid == 0) atom_add(p.ws, OFF_CG + (s - 2) * 128, 1); hload(s, 1); }
      else if (s + 2 < NSEG) { wait_flags(ws, foff == OOB ? OOB : foff + (s + 2) * 128, ftgt); hload(s + 2, 0); }
      LAS char* xb = smem + hf * 49152;
      bf16x8 b[3][2];
#pragma unroll
      for (int q = 0; q < 2; q++) { b[q][0] = *(LAS bf16x8*)(xb + (q * 2) * 1024 + lane * 16); b[q][1] = *(LAS bf16x8*)(xb + (q * 2 + 1) * 1024 + lane * 16); }
#pragma unroll
      for (int kk = 0; kk < 24; kk++) {
        if (kk + 2 < 24) { b[(kk + 2) % 3][0] = *(LAS bf16x8*)(xb + ((kk + 2) * 2) * 1024 + lane * 16); b[(kk + 2) % 3][1] = *(LAS bf16x8*)(xb + ((kk + 2) * 2 + 1) * 1024 + lane * 16); }
        SB();
        const int kg = hf * 24 + kk;
#pragma unroll
        for (int t = 0; t < 2; t++)
#pragma unroll
          for (int tt = 0; tt < 2; tt++) {
            if (kg == 0) mf0<true>(acc[t][tt], W[t][kg], b[kk % 3][tt]);
            else if (t * 48 + kg < 64) mfa<true>(acc[t][tt], W[t][kg], b[kk % 3][tt]);
            else mfa<false>(acc[t][tt], W[t][kg], b[kk % 3][tt]);
          }
        SB();
      }
    }
    asm volatile("s_nop 7" ::: "memory");
#pragma unroll
    for (int tt = 0; tt < 2; tt++) {
      asm volatile("" : "+v"(acc[0][tt]), "+v"(acc[1][tt]));
      f32x4 g0, g1;
#pragma unroll
      for (int r = 0; r < 4; r++) {
        float d0 = rbf(acc[0][tt][r]), d1 = rbf(acc[1][tt][r]);
        g0[r] = d0 / (1.0f + __expf(-d0)); g1[r] = d1 / (1.0f + __expf(-d1));
      }
      const int tok = 32 * s + 8 * (n >> 2) + 4 * tt + (n & 3);
      __builtin_amdgcn_raw_buffer_store_b128(pack8(g0, g1), ws, OFF_G + tok * 1280 + (128 * c + 32 * w + 8 * g) * 2, 0, 16);
    }
  }
  drain(); __syncthreads();
  if (tid == 0) atom_add(p.ws, OFF_CG + (NSEG - 2 + par) * 128, 1);

}

constexpr int NJOIN = 16;
DEVI void token_A(const Params& p, int s, int h, rsrc_t ws, int lane) {
  const int n = lane & 15, g = lane >> 4;
  wait_flags_slow(ws, lane == 0 ? (unsigned)((s * 128 + h) * 4) : lane == 1 ? (unsigned)((s * 128 + 32 + h) * 4) : OOB, lane < 2 ? 6u : 0u);
  bf16x8 qf[2][6], kf[2][6], km[6];
#pragma unroll
  for (int ks = 0; ks < 6; ks++)
#pragma unroll
    for (int tt = 0; tt < 2; tt++) {
      qf[tt][ks] = __builtin_bit_cast(bf16x8, ldx(ws, lane * 16, OFF_QF + ((s * 192 + h * 6 + ks) * 2 + tt) * 1024));
      kf[tt][ks] = __builtin_bit_cast(bf16x8, ldx(ws, lane * 16, OFF_KF + ((s * 192 + h * 6 + ks) * 2 + tt) * 1024));
    }
  SB();
  wait_flags_slow(ws, lane == 0 ? (unsigned)(OFF_KMF + (s * 32 + h) * 128) : OOB, lane == 0 ? 1u : 0u, s >= NSEG - 1);
#pragma unroll
  for (int ks = 0; ks < 6; ks++) km[ks] = __builtin_bit_cast(bf16x8, ldx(ws, g * 128 + (n & 7) * 16, OFF_KM + ((s * 32 + h) * 6 + ks) * 512));
  const float cs = 0.07216878364870322f * 1.4426950408889634f;
#pragma unroll
  for (int tq = 0; tq < 2; tq++) {
    f32x4 T0 = {0, 0, 0, 0}, T1 = {0, 0, 0, 0}, T2 = {0, 0, 0, 0};
#pragma unroll
    for (int ks = 0; ks < 6; ks++) {
      T0 = __builtin_amdgcn_mfma_f32_16x16x32_bf16(km[ks], qf[tq][ks], T0, 0, 0, 0);
      T1 = __builtin_amdgcn_mfma_f32_16x16x32_bf16(kf[0][ks], qf[tq][ks], T1, 0, 0, 0);
      T2 = __builtin_amdgcn_mfma_f32_16x16x32_bf16(kf[1][ks], qf[tq][ks], T2, 0, 0, 0);
    }
    const int qt = 8 * (n >> 2) + 4 * tq + (n & 3);
    float m = fmaxf(fmaxf(T0[0], T0[1]), fmaxf(T0[2], T0[3]));
#pragma unroll
    for (int r = 0; r < 4; r++) {
      if (8 * g + r > qt) T1[r] = -3.0e38f;
      if (8 * g + 4 + r > qt) T2[r] = -3.0e38f;
      m = fmaxf(m, fmaxf(T1[r], T2[r]));
    }
    m = xor32max(xor16max(m));
    float l0 = 0, lx = 0; f32x4 E0, E1, E2;
#pragma unroll
    for (int r = 0; r < 4; r++) {
      E0[r] = __builtin_amdgcn_exp2f((T0[r] - m) * cs);
      E1[r] = (8 * g + r > qt) ? 0.f : __builtin_amdgcn_exp2f((T1[r] - m) * cs);
      E2[r] = (8 * g + 4 + r > qt) ? 0.f : __builtin_amdgcn_exp2f((T2[r] - m) * cs);
      l0 += E0[r]; lx += E1[r] + E2[r];
    }
    const float inv = 1.0f / (xor16add(l0) + xor32add(xor16add(lx)));
    E0 *= inv; E1 *= inv; E2 *= inv;
    const unsigned so = OFF_PX + (s * 160 + h * 5) * 512 + tq * 256;
    __builtin_amdgcn_raw_buffer_store_b64((u32x2){pk2(E0[0], E0[1]), pk2(E0[2], E0[3])}, ws, g < 2 ? (unsigned)(n * 16 + g * 8) : OOB, so, 16);
    __builtin_amdgcn_raw_buffer_store_b128(pack8(E1, E2), ws, (1 + g) * 512 + n * 16, so, 16);
  }
  drain();
  if (lane == 0) atom_add(p.ws, OFF_CPX + s * 128, 1);
}

DEVI void token_B(const Params& p, int s, int cg, LAS char* smem, rsrc_t ws, int tid, int lane, int w) {
  const int n = lane & 15, g = lane >> 4;
  const unsigned zb = (16 * cg + 4 * w + (n >> 3)) * 20480 + (n & 7) * 16;
  const unsigned zx = zb + (1 + g) * 128, zy = ((16 * cg + 4 * w + (n >> 3)) * 32 + g) * 128 + (n & 7) * 16;
  const unsigned zs = OFF_Z + s * 80 * 20480;
  if (w == 0) wait_flags_slow(ws, lane == 0 ? (unsigned)(OFF_REC + s * 128) : lane == 1 ? (unsigned)(OFF_CG + s * 128) : OOB, lane == 0 ? 32u : lane == 1 ? 5u : 0u);
  __syncthreads();
  bf16x8 z[40][2];
#pragma unroll
  for (int q = 0; q < 28; q++)
#pragma unroll
    for (int u = 0; u < 2; u++) z[q][u] = __builtin_bit_cast(bf16x8, ldx(ws, zx + u * 2 * 20480 + q * 640, zs));
  u32x2 xv[2][2], gv[2][2];
#pragma unroll
  for (int u = 0; u < 2; u++)
#pragma unroll
    for (int tq = 0; tq < 2; tq++) {
      const int tok = 32 * s + 8 * (n >> 2) + 4 * tq + (n & 3);
      const int col = 128 * cg + 32 * w + 16 * u + 4 * g;
      xv[u][tq] = *(const u32x2*)(p.x + (size_t)tok * 640 + col);
      gv[u][tq] = __builtin_amdgcn_raw_buffer_load_b64(ws, OFF_G + tok * 1280 + col * 2, 0, 16);
    }
  SB();
  if (w == 0) {
    if (s == NSEG - 1) {
      wait_flags_slow(ws, OFF_YF + s * 512 + lane * 4, 1u, true);
      wait_flags_slow(ws, lane < 16 ? (unsigned)(OFF_YF + s * 512 + 256 + lane * 4) : OOB, lane < 16 ? 1u : 0u, true);
    }
    wait_flags_slow(ws, lane == 0 ? (unsigned)(OFF_CPX + s * 128) : (lane == 1 && s + 1 < NSEG) ? (unsigned)(OFF_CPX + (s + 1) * 128) : OOB, lane == 0 ? 32u : (lane == 1 && s + 1 < NSEG) ? 1u : 0u, s >= NSEG - 2);
  }
  __syncthreads();
  u32x4 st[20];
#pragma unroll
  for (int i = 0; i < 20; i++) st[i] = ldx(ws, (i * 256 + tid) * 16, OFF_PX + s * 81920);
#pragma unroll
  for (int q = 28; q < 32; q++)
#pragma unroll
    for (int u = 0; u < 2; u++) z[q][u] = __builtin_bit_cast(bf16x8, ldx(ws, zx + u * 2 * 20480 + q * 640, zs));
#pragma unroll
  for (int q = 32; q < 40; q++)
#pragma unroll
    for (int u = 0; u < 2; u++) z[q][u] = __builtin_bit_cast(bf16x8, ldx(ws, zy + u * 2 * 4096 + (q - 32) * 512, OFF_YB + s * (80 * 4096)));
  SB();
#pragma unroll
  for (int i = 0; i < 20; i++) *(LAS u32x4*)(smem + (i * 256 + tid) * 16) = st[i];
  __syncthreads();
  f32x4 acc[2][2] = {{{0, 0, 0, 0}, {0, 0, 0, 0}}, {{0, 0, 0, 0}, {0, 0, 0, 0}}};
#pragma unroll
  for (int ks = 0; ks < 40; ks++) {
    const int cb = ks < 32 ? (5 * ks + 1) * 512 + g * 512 : (20 * (ks - 32)) * 512 + g * 2560;
    bf16x8 b0 = *(LAS bf16x8*)(smem + cb + n * 16), b1 = *(LAS bf16x8*)(smem + cb + 256 + n * 16);
    acc[0][0] = __builtin_amdgcn_mfma_f32_16x16x32_bf16(z[ks][0], b0, acc[0][0], 0, 0, 0);
    acc[0][1] = __builtin_amdgcn_mfma_f32_16x16x32_bf16(z[ks][0], b1, acc[0][1], 0, 0, 0);
    acc[1][0] = __builtin_amdgcn_mfma_f32_16x16x32_bf16(z[ks][1], b0, acc[1][0], 0, 0, 0);
    acc[1][1] = __builtin_amdgcn_mfma_f32_16x16x32_bf16(z[ks][1], b1, acc[1][1], 0, 0, 0);
  }
#pragma unroll
  for (int u = 0; u < 2; u++)
#pragma unroll
    for (int tq = 0; tq < 2; tq++) {
      const int tok = 32 * s + 8 * (n >> 2) + 4 * tq + (n & 3);
      const int col = 128 * cg + 32 * w + 16 * u + 4 * g;
      float o[4];
#pragma unroll
      for (int r = 0; r < 4; r++) {
        unsigned xw = r < 2 ? xv[u][tq].x : xv[u][tq].y, gw = r < 2 ? gv[u][tq].x : gv[u][tq].y;
        float xf = __uint_as_float((r & 1) ? (xw & 0xffff0000u) : (xw << 16)), gf = __uint_as_float((r & 1) ? (gw & 0xffff0000u) : (gw << 16));
        o[r] = xf + rbf(rbf(acc[u][tq][r]) * gf);
      }
      *(u32x2*)(p.out + (size_t)tok * 640 + col) = (u32x2){pk2(o[0], o[1]), pk2(o[2], o[3])};
    }
  __syncthreads();
}

template <bool ISB>
DEVI void role_TK(const Params& p, const int wv, LAS char* smem, rsrc_t ws) {
  int tid = wv * 64 + lane_id(); asm volatile("; roleTK" : "+v"(tid) :: "memory");
  const int lane = tid & 63, w = wv;
  LAS unsigned* qw = (LAS unsigned*)(smem + 98304);
  unsigned* head = (unsigned*)(p.ws + OFF_TQ + (ISB ? 128 : 0));
  while (true) {
    __syncthreads();
    if (tid == 0) *qw = __hip_atomic_fetch_add(head, 1u, __ATOMIC_RELAXED, __HIP_MEMORY_SCOPE_AGENT);
    __syncthreads();
    const int idx = __builtin_amdgcn_readfirstlane((int)*qw);
    if (idx >= NSEG * (ISB ? 5 : 8)) break;
    if constexpr (ISB) token_B(p, idx / 5, idx % 5, smem, ws, tid, lane, w);
    else token_A(p, idx >> 3, 4 * (idx & 7) + w, ws, lane);
  }
  __builtin_amdgcn_endpgm(); __builtin_unreachable();
}

constexpr int PB = 512;
DEVI float wave_sum(float v) {
  v += __uint_as_float(__builtin_amdgcn_update_dpp(0u, __float_as_uint(v), 0x121, 0xf, 0xf, false));
  v += __uint_as_float(__builtin_amdgcn_update_dpp(0u, __float_as_uint(v), 0x122, 0xf, 0xf, false));
  v += __uint_as_float(__builtin_amdgcn_update_dpp(0u, __float_as_uint(v), 0x124, 0xf, 0xf, false));
  v += __uint_as_float(__builtin_amdgcn_update_dpp(0u, __float_as_uint(v), 0x128, 0xf, 0xf, false));
  v = xor32add(xor16add(v));
  return __uint_as_float(__builtin_amdgcn_readfirstlane(__float_as_uint(v)));
}
DEVI void ln_row(const Params& p, int tok, u32x4 v0, u32x4 v1, int lane) {
  float f[16];
#pragma unroll
  for (int i = 0; i < 4; i++) {
    unsigned a = v0[i], c = v1[i];
    f[2 * i] = __uint_as_float(a << 16); f[2 * i + 1] = __uint_as_float(a & 0xffff0000u);
    f[8 + 2 * i] = __uint_as_float(c << 16); f[8 + 2 * i + 1] = __uint_as_float(c & 0xffff0000u);
  }
  float sum = 0;
#pragma unroll
  for (int i = 0; i < 16; i++) sum += f[i];
  sum = wave_sum(sum);
  const float mean = sum * (1.0f / 640.0f);
  float sq = 0;
#pragma unroll
  for (int i = 0; i < 8; i++) { float d = f[i] - mean; sq += d * d; }
  if (lane < 16) {
#pragma unroll
    for (int i = 8; i < 16; i++) { float d = f[i] - mean; sq += d * d; }
  }
  sq = wave_sum(sq);
  const float mb = rbf(mean), vb = rbf(sq * (1.0f / 640.0f));
  const float den = rbf(sqrtf(rbf(vb + 1e-5f)));
  float y[16];
#pragma unroll
  for (int i = 0; i < 16; i++) y[i] = rbf(f[i] - mb) / den;
  const int s = tok >> 5, t = tok & 31, tt = (t >> 2) & 1, n = 4 * (t >> 3) + (t & 3);
  int pc = lane; int cb = pc >> 2, g = pc & 3;
  *(u32x4*)(p.ws + OFF_XNF + ((s * 20 + cb) * 2 + tt) * 1024 + (n + 16 * g) * 16) = (u32x4){pk2(y[0], y[1]), pk2(y[2], y[3]), pk2(y[4], y[5]), pk2(y[6], y[7])};
  if (lane < 16) { pc = 64 + lane; cb = pc >> 2; g = pc & 3;
    *(u32x4*)(p.ws + OFF_XNF + ((s * 20 + cb) * 2 + tt) * 1024 + (n + 16 * g) * 16) = (u32x4){pk2(y[8], y[9]), pk2(y[10], y[11]), pk2(y[12], y[13]), pk2(y[14], y[15])}; }
}
__global__ __launch_bounds__(256) void k_init(Params p) {
  const int tid = threadIdx.x, lane = tid & 63, w = tid >> 6;
  const int b = blockIdx.x;
  const uint16_t* xr = p.x + (size_t)(b * 4 + w) * 640;
  const u32x4 v0 = *(const u32x4*)(xr + lane * 8);
  const u32x4 v1r = *(const u32x4*)(xr + 512 + (lane & 15) * 8);
  const u32x4 v1 = lane < 16 ? v1r : (u32x4){0, 0, 0, 0};
  const u32x4 ones = {SENT, SENT, SENT, SENT}, zero = {0, 0, 0, 0};
  for (unsigned o = OFF_MEMB + 10240 + (b * 256 + tid) * 16; o < SENT_END; o += PB * 256 * 16) *(u32x4*)(p.ws + o) = ones;
  for (unsigned o = (b * 256 + tid) * 16; o < FLAGS_END; o += PB * 256 * 16) *(u32x4*)(p.ws + o) = zero;
  if (b == PB - 1) {
    for (int u = tid; u < 640; u += 256) {
      int j = u >> 3, i = u & 7;
      u32x4 v = *(const u32x4*)(p.mem0 + i * 640 + j * 8);
      v.x = san(v.x); v.y = san(v.y); v.z = san(v.z); v.w = san(v.w);
      *(u32x4*)(p.ws + OFF_MEMB + j * 128 + i * 16) = v;
    }
  }
  ln_row(p, b * 4 + w, v0, v1, lane);
}

DEVI void b_task(const Params& p, const int wv, int h, int nb, LAS char* sm, rsrc_t ws) {
  int tid = wv * 64 + lane_id(); asm volatile("; btask" : "+v"(tid) :: "memory");
  const int lane = tid & 63, w = wv;
  const int m = lane & 15, g = lane >> 4;
  u32x4 t15[15];
#pragma unroll
  for (int i = 0; i < 15; i++) { int u = i * 256 + tid; int row = u / 20, pc = u - row * 20; t15[i] = *(const u32x4*)(p.Wo + ((size_t)h * 192 + row) * 640 + 160 * nb + pc * 8); }
  bf16x8 an[4][6];
  rsrc_t rv = __builtin_amdgcn_make_buffer_rsrc((void*)p.Wv, 0, 640 * 6144 * 2, 0x00020000);
  const unsigned avo = m * 12288 + h * 384 + g * 16;
#pragma unroll
  for (int mt = 0; mt < 4; mt++)
#pragma unroll
    for (int ks = 0; ks < 6; ks++) an[mt][ks] = __builtin_bit_cast(bf16x8, __builtin_amdgcn_raw_buffer_load_b128(rv, avo + mt * (16 * 12288) + ks * 64, w * 64 * 12288, 0));
#pragma unroll
  for (int i = 0; i < 15; i++) { int u = i * 256 + tid; int row = u / 20, pc = u - row * 20; *(LAS u32x4*)(sm + row * 336 + pc * 16) = t15[i]; }
  __syncthreads();
  bf16x8 Bf[10][6];
  const int p4 = lane & 3;
#pragma unroll
  for (int nt = 0; nt < 10; nt++)
#pragma unroll
    for (int ks = 0; ks < 6; ks++) Bf[nt][ks] = tr8(sm, 336, 32 * ks, (16 * nt + 4 * p4) * 2, lane);
  const unsigned bo = (((160 * nb + m) >> 3) * 16 + (h >> 1)) * 20480 + ((h & 1) * 8 + (m & 7) + 16 * g) * 16;
#pragma unroll 1
  for (int kk = w; kk < 10; kk += 4) {
    const int kn = kk + 4 < 10 ? kk + 4 : kk;
#pragma unroll
    for (int hf = 0; hf < 4; hf++) {
      const int u = hf & 1, c5 = (hf >> 1) * 5;
      f32x4 acc[2][5];
#pragma unroll
      for (int ks = 0; ks < 6; ks++)
#pragma unroll
        for (int a = 0; a < 2; a++)
#pragma unroll
          for (int nt = 0; nt < 5; nt++) acc[a][nt] = __builtin_amdgcn_mfma_f32_16x16x32_bf16(an[u + 2 * a][ks], Bf[c5 + nt][ks], ks == 0 ? (f32x4){0, 0, 0, 0} : acc[a][nt], 0, 0, 0);
#pragma unroll
      for (int nt = 0; nt < 5; nt++) {
        const f32x4 &A = acc[0][nt], &B = acc[1][nt];
        u32x4 v = {pk2(A[0], A[1]), pk2(B[0], B[1]), pk2(A[2], A[3]), pk2(B[2], B[3])};
        __builtin_amdgcn_raw_buffer_store_b128(v, ws, bo + (c5 + nt) * (2 * 16 * 20480) + u * 1024, OFF_BF + kk * 2048, 16);
      }
      if (hf >= 2) {
#pragma unroll
        for (int a = 0; a < 2; a++)
#pragma unroll
          for (int ks = 0; ks < 6; ks++) an[u + 2 * a][ks] = __builtin_bit_cast(bf16x8, __builtin_amdgcn_raw_buffer_load_b128(rv, avo + (u + 2 * a) * (16 * 12288) + ks * 64, kn * 64 * 12288, 0));
      }
      SB();
    }
  }
  drain(); __syncthreads();
  if (tid == 0) atom_add(p.ws, OFF_BRDY + nb * 128, 1);
}

__global__ __launch_bounds__(256, 1) void k_main(Params p) {
  extern __shared__ __attribute__((aligned(16))) char smem_[];
  LAS char* smem = (LAS char*)smem_;
  rsrc_t ws = __builtin_amdgcn_make_buffer_rsrc(p.ws, 0, 0x7fffffff, 0x00020000);
  const int b = blockIdx.x;
  const int wv = __builtin_amdgcn_readfirstlane(threadIdx.x >> 6);
  const int x = b & 7, r = b >> 3;
  const int sl2 = r * 4 + (x - 4);
  {
    int e = 99, hb = 0;
    if (x < 4) { e = r >= 28 ? r - 28 : r >= 8 ? r - 4 : 99; if (e >= 20) e = 99; hb = 12 + 5 * x; }
    else { const int xq = x - 4, nq = (19 - xq + 3) >> 2; e = sl2 < 19 ? r : sl2 >= 90 ? nq + ((sl2 - 90 - ((xq - 90) & 3)) >> 2) : 99; if (e >= 12) e = 99; hb = 3 * xq; }
    if (e < 99) b_task(p, wv, hb + (e >> 2), e & 3, smem, ws);
  }
  if (x < 4) {
    if (r < 8) role_S(p, wv, r * 4 + x, smem, ws);
    if (r < 28) role_V(p, wv, (r - 8) * 4 + x, smem, ws);
    role_TK<false>(p, wv, smem, ws);
  } else {
    if (sl2 < N_QKV) { role_QKV(p, wv, sl2, smem, ws); return; }
    else if (sl2 < N_QKV + N_VO) { role_VO(p, wv, sl2 - N_QKV, smem, ws); if (sl2 - N_QKV >= NJOIN) return; }
    else if (sl2 < N_QKV + N_VO + N_GU) { role_GU(p, wv, sl2 - N_QKV - N_VO, smem, ws); return; }
    else if (sl2 < N_QKV + N_VO + N_GU + N_DN) { role_DN(p, wv, sl2 - N_QKV - N_VO - N_GU, smem, ws); return; }
    role_TK<true>(p, wv, smem, ws);
  }
}

extern "C" void kernel_launch(void* const* d_in, const int* in_sizes, int n_in, void* d_out, int out_size, void* d_ws, size_t ws_size, hipStream_t stream) {
  Params p;
  p.x = (const uint16_t*)d_in[0]; p.mem0 = (const uint16_t*)d_in[1];
  p.Wq = (const uint16_t*)d_in[2]; p.Wk = (const uint16_t*)d_in[3]; p.Wv = (const uint16_t*)d_in[4]; p.Wo = (const uint16_t*)d_in[5];
  p.Wg = (const uint16_t*)d_in[8]; p.Wu = (const uint16_t*)d_in[9]; p.Wd = (const uint16_t*)d_in[10];
  p.out = (uint16_t*)d_out; p.ws = (char*)d_ws;
  (void)hipFuncSetAttribute((const void*)k_main, hipFuncAttributeMaxDynamicSharedMemorySize, 160 * 1024);
  k_init<<<PB, 256, 0, stream>>>(p);
  k_main<<<256, 256, 160 * 1024, stream>>>(p);
}
```
